```python
import torch

def _softmax(x, axis=-1):
    return torch.softmax(x, dim=axis)
def _gelu(x):
    return torch.nn.functional.gelu(x, approximate="tanh")
def _silu(x):
    return torch.nn.functional.silu(x)
def _sigmoid(x):
    return torch.sigmoid(x)
def _layernorm(x, axis=-1, eps=1e-5):
    m = x.mean(dim=axis, keepdim=True)
    v = x.var(dim=axis, keepdim=True, unbiased=False)
    return (x - m) / torch.sqrt(v + eps)
def _scatter_add(tgt, idx, src, axis=0):
    return tgt.clone().index_add_(axis, idx, src)
def _segment_sum(src, seg, n):
    out = torch.zeros((n,) + src.shape[1:], dtype=src.dtype)
    return out.index_add_(0, seg, src)
def _pad(x, pw):
    flat = tuple(v for pair in reversed(pw) for v in pair)
    return torch.nn.functional.pad(x, flat)

kernel_name = 'sequential_preLN__spectral_gfnet__dense_activation_mish__prompt'

def setup_inputs():

    _w = (lambda a: (lambda t: t.to(torch.bfloat16) if t.is_floating_point() else t)(torch.as_tensor(a)))
    def _A_setup():
        B, S, D = 1, 2048, 1024
        Df = D // 2 + 1
        return {
            "x": _w((lambda *s: torch.randn(*s, dtype=torch.bfloat16) * 0.05)(B, S, D)),
            "w_r": _w((lambda *s: torch.randn(*s, dtype=torch.bfloat16) * 0.4)(S, Df) * 0.02),
            "w_i": _w((lambda *s: torch.randn(*s, dtype=torch.bfloat16) * 0.4)(S, Df) * 0.02),
        }
    _A_in = _A_setup()

    def _B_setup():
        B, S, D, F = 1, 2048, 1024, 3584
        return {
            "x": _w((lambda *s: torch.randn(*s, dtype=torch.bfloat16) * 0.05)(B, S, D)),
            "w1": _w((lambda *s: torch.randn(*s, dtype=torch.bfloat16) * 0.8)(D, F) * (D ** -0.5)),
            "b1": _w((lambda *s: torch.randn(*s, dtype=torch.bfloat16) * 0.2)(F)),
            "w2": _w((lambda *s: torch.randn(*s, dtype=torch.bfloat16) * 0.1)(F, D) * (F ** -0.5)),
        }
    _B_in = _B_setup()

    return {
        "A_x": _A_in["x"],
        "A_w_r": _A_in["w_r"],
        "A_w_i": _A_in["w_i"],
        "B_x": _B_in["x"],
        "B_w1": _B_in["w1"],
        "B_b1": _B_in["b1"],
        "B_w2": _B_in["w2"],
    }

def reference(A_x, A_w_r, A_w_i, B_x, B_w1, B_b1, B_w2):
    def _A_ref(x, w_r, w_i):
        B, S, D = x.shape
        X = (lambda x, axes=(-2,-1): torch.fft.rfft2(x.float(), dim=axes))(x, axes=(-2, -1))
        W = w_r + 1j * w_i
        X = X * W
        y = (lambda x, s, axes=(-2,-1): torch.fft.irfft2(x, s=s, dim=axes).to(torch.bfloat16))(X, (S, D), axes=(-2, -1))
        return y

    def _B_ref(x, w1, b1, w2):
        h = torch.einsum("bsd,df->bsf", x, w1) + b1
        sp = torch.log1p(torch.exp(-torch.abs(h))) + torch.where(h > 0, h, torch.zeros_like(h))
        act = h * torch.tanh(sp)
        return torch.einsum("bsf,fd->bsd", act, w2)

    x = A_x
    h = x + _A_ref(_layernorm(x, axis=-1), A_w_r, A_w_i)
    h2 = h + _B_ref(_layernorm(h, axis=-1), B_w1, B_b1, B_w2)
    return h2
```

```cpp
#include <hip/hip_runtime.h>
#include <hip/hip_bf16.h>

typedef unsigned short u16;
typedef unsigned int u32;
typedef short s16x4 __attribute__((ext_vector_type(4)));
typedef short s16x8 __attribute__((ext_vector_type(8)));
typedef __bf16 bf16x8 __attribute__((ext_vector_type(8)));
typedef float f32x4 __attribute__((ext_vector_type(4)));
typedef u32 u32x2 __attribute__((ext_vector_type(2)));
#define LDS_AS __attribute__((address_space(3)))

constexpr int S_ = 2048, D_ = 1024, F_ = 3584, DF_ = 513;

__device__ __forceinline__ float bf2f(u16 v) { return __uint_as_float(((u32)v) << 16); }
__device__ __forceinline__ u16 f2bf(float f) { return __builtin_bit_cast(u16, (__bf16)f); }
__device__ __forceinline__ float bfr(float f) { return bf2f(f2bf(f)); }
__device__ __forceinline__ u32 pk2(float lo, float hi) { return (u32)f2bf(lo) | ((u32)f2bf(hi) << 16); }

__device__ __forceinline__ float2 cadd(float2 a, float2 b) { return make_float2(a.x + b.x, a.y + b.y); }
__device__ __forceinline__ float2 csub(float2 a, float2 b) { return make_float2(a.x - b.x, a.y - b.y); }
__device__ __forceinline__ float2 cmul(float2 a, float2 b) { return make_float2(a.x * b.x - a.y * b.y, a.x * b.y + a.y * b.x); }
__device__ __forceinline__ float2 cmulc(float2 a, float2 b) { return make_float2(a.x * b.x + a.y * b.y, a.y * b.x - a.x * b.y); }
template <int SGN> __device__ __forceinline__ float2 twm(float2 a, float2 w) { return SGN < 0 ? cmul(a, w) : cmulc(a, w); }
template <int SGN> __device__ __forceinline__ float2 muli(float2 a) { return SGN < 0 ? make_float2(a.y, -a.x) : make_float2(-a.y, a.x); }

template <int SGN> __device__ __forceinline__ void dft4(float2& b0, float2& b1, float2& b2, float2& b3) {
  float2 s02 = cadd(b0, b2), d02 = csub(b0, b2), s13 = cadd(b1, b3), d13 = muli<SGN>(csub(b1, b3));
  b0 = cadd(s02, s13); b2 = csub(s02, s13); b1 = cadd(d02, d13); b3 = csub(d02, d13);
}
template <int SGN> __device__ __forceinline__ void dft8(float2 (&a)[8]) {
  const float R = 0.70710678118654752440f;
  float2 e0 = cadd(a[0], a[4]), e1 = cadd(a[1], a[5]), e2 = cadd(a[2], a[6]), e3 = cadd(a[3], a[7]);
  float2 o0 = csub(a[0], a[4]), o1 = csub(a[1], a[5]), o2 = csub(a[2], a[6]), o3 = csub(a[3], a[7]);
  if (SGN < 0) {
    o1 = make_float2((o1.x + o1.y) * R, (o1.y - o1.x) * R);
    o3 = make_float2((o3.y - o3.x) * R, -(o3.x + o3.y) * R);
  } else {
    o1 = make_float2((o1.x - o1.y) * R, (o1.x + o1.y) * R);
    o3 = make_float2(-(o3.x + o3.y) * R, (o3.x - o3.y) * R);
  }
  o2 = muli<SGN>(o2);
  dft4<SGN>(e0, e1, e2, e3);
  dft4<SGN>(o0, o1, o2, o3);
  a[0] = e0; a[2] = e1; a[4] = e2; a[6] = e3;
  a[1] = o0; a[3] = o1; a[5] = o2; a[7] = o3;
}

__device__ __forceinline__ float wave_sum(float v) {
#pragma unroll
  for (int o = 32; o >= 1; o >>= 1) v += __shfl_xor(v, o);
  return v;
}

template <int SGN> __device__ __forceinline__ void fft512_wave(float2 (&a)[8], float2* bw, const float2* tw, int lane) {
  dft8<SGN>(a);
#pragma unroll
  for (int m = 1; m < 8; ++m) a[m] = twm<SGN>(a[m], tw[(2 * lane * m) & 1023]);
#pragma unroll
  for (int m = 0; m < 8; ++m) bw[m * 72 + lane] = a[m];
  __builtin_amdgcn_s_waitcnt(0xC07F);
  __builtin_amdgcn_wave_barrier();
  const int l0 = lane & 7, mm = lane >> 3;
#pragma unroll
  for (int j = 0; j < 8; ++j) a[j] = bw[mm * 72 + l0 + 8 * j];
  __builtin_amdgcn_s_waitcnt(0xC07F);
  __builtin_amdgcn_wave_barrier();
  dft8<SGN>(a);
#pragma unroll
  for (int m = 1; m < 8; ++m) a[m] = twm<SGN>(a[m], tw[(16 * l0 * m) & 1023]);
#pragma unroll
  for (int m = 0; m < 8; ++m) bw[l0 * 66 + mm + 8 * m] = a[m];
  __builtin_amdgcn_s_waitcnt(0xC07F);
  __builtin_amdgcn_wave_barrier();
#pragma unroll
  for (int j = 0; j < 8; ++j) a[j] = bw[j * 66 + lane];
  __builtin_amdgcn_s_waitcnt(0xC07F);
  __builtin_amdgcn_wave_barrier();
  dft8<SGN>(a);
}

constexpr int RS_ = 584;

__device__ __forceinline__ int k2_freq(int i, int tau) {
  const int h = tau & 3, m2 = (tau >> 2) & 7, m1 = tau >> 5, e = i >> 2, m4 = i & 3;
  return m1 + 8 * m2 + 64 * (2 * h + e) + 512 * m4;
}

__global__ __launch_bounds__(512) void k1_ln_rfft(const u16* __restrict__ x, const u16* __restrict__ wr, const u16* __restrict__ wi,
                                                  float2* __restrict__ T, u32* __restrict__ Wt) {
  __shared__ __attribute__((aligned(16))) float2 tw[1024];
  __shared__ __attribute__((aligned(16))) float2 buf[8 * RS_];
  __shared__ u32 wsm[8][516];
  const int tid = threadIdx.x, lane = tid & 63, w = tid >> 6, blk = blockIdx.x;
  const int row = blk * 8 + w;
  const u32* xr = (const u32*)(x + (size_t)row * D_);
  u32 raw[8];
#pragma unroll
  for (int j = 0; j < 8; ++j) raw[j] = xr[lane + 64 * j];
  {
    const int f = k2_freq(blk >> 5, ((blk & 31) << 3) + w);
    const u16* pr = wr + (size_t)f * DF_;
    const u16* pi = wi + (size_t)f * DF_;
#pragma unroll
    for (int j = 0; j < 8; ++j) { const int k = lane + 64 * j; wsm[w][k] = (u32)pr[k] | ((u32)pi[k] << 16); }
  }
  for (int j = tid; j < 1024; j += 512) { float s, c; sincospif(-(float)j * (1.0f / 512.0f), &s, &c); tw[j] = make_float2(c, s); }
  float2 a[8];
  float sum = 0.f;
#pragma unroll
  for (int j = 0; j < 8; ++j) { a[j] = make_float2(__uint_as_float(raw[j] << 16), __uint_as_float(raw[j] & 0xffff0000u)); sum += a[j].x + a[j].y; }
  const float mean = wave_sum(sum) * (1.0f / 1024.0f);
  float sq = 0.f;
#pragma unroll
  for (int j = 0; j < 8; ++j) { const float dx = a[j].x - mean, dy = a[j].y - mean; sq += dx * dx + dy * dy; }
  const float var = wave_sum(sq) * (1.0f / 1024.0f);
  const float mb = bfr(mean), den = bfr(sqrtf(bfr(bfr(var) + 1e-5f)));
#pragma unroll
  for (int j = 0; j < 8; ++j) { a[j].x = bfr(bfr(a[j].x - mb) / den); a[j].y = bfr(bfr(a[j].y - mb) / den); }
  __syncthreads();
  float2* bw = buf + w * RS_;
  fft512_wave<-1>(a, bw, tw, lane);
#pragma unroll
  for (int m = 0; m < 8; ++m) bw[lane + 64 * m] = a[m];
  __syncthreads();
  const int r = tid & 7;
  const float2* zr = buf + r * RS_;
#pragma unroll
  for (int it = 0; it < 4; ++it) {
    const int kp = (tid >> 3) + 64 * it;
    float4 o;
#pragma unroll
    for (int e = 0; e < 2; ++e) {
      const int k = 2 * kp + e;
      const float2 zk = zr[k], zm = zr[(512 - k) & 511];
      const float2 sum2 = make_float2(zk.x + zm.x, zk.y - zm.y);
      const float2 dif2 = make_float2(zk.x - zm.x, zk.y + zm.y);
      const float2 t = cmul(dif2, tw[k]);
      float2 X = make_float2(sum2.x + t.y, sum2.y - t.x);
      if (k == 0) X = make_float2(2.f * (zk.x + zk.y), 2.f * (zk.x - zk.y));
      if (e == 0) { o.x = X.x; o.y = X.y; } else { o.z = X.x; o.w = X.y; }
    }
    *(float4*)(T + ((size_t)kp * S_ + blk * 8 + r) * 2) = o;
  }
  {
    const int i = blk >> 5, tau0 = (blk & 31) << 3;
#pragma unroll
    for (int it = 0; it < 4; ++it) {
      const int kp = (tid >> 3) + 64 * it;
      u32x2 v; v.x = wsm[r][2 * kp]; v.y = wsm[r][2 * kp + 1];
      *(u32x2*)(Wt + ((size_t)kp * 8 + i) * 512 + 2 * (tau0 + r)) = v;
    }
  }
}

__global__ __launch_bounds__(512) void k2_col(float2* __restrict__ T, const u32* __restrict__ Wt, const u16* __restrict__ wr, const u16* __restrict__ wi) {
  __shared__ __attribute__((aligned(16))) float2 tw[2048];
  __shared__ __attribute__((aligned(16))) float2 ex[4608];
  const int t = threadIdx.x, kp = blockIdx.x, c = t & 1, u = t >> 1;
  float2* col = T + (size_t)kp * S_ * 2;
  float2 a[8];
#pragma unroll
  for (int j = 0; j < 8; ++j) a[j] = col[(u + 256 * j) * 2 + c];
  u32 wv[8];
#pragma unroll
  for (int i = 0; i < 8; ++i) wv[i] = Wt[((size_t)kp * 8 + i) * 512 + t];
  for (int j = t; j < 2048; j += 512) { float s, cc; sincospif(-(float)j * (1.0f / 1024.0f), &s, &cc); tw[j] = make_float2(cc, s); }
  __syncthreads();
  const int u0 = u & 31, m1 = u >> 5;
  const int v0 = u & 3, g = u >> 2;
  dft8<-1>(a);
#pragma unroll
  for (int m = 1; m < 8; ++m) a[m] = cmul(a[m], tw[(u * m) & 2047]);
#pragma unroll
  for (int m = 0; m < 8; ++m) ex[(m * 256 + u) * 2 + c] = a[m];
  __syncthreads();
#pragma unroll
  for (int j = 0; j < 8; ++j) a[j] = ex[(m1 * 256 + u0 + 32 * j) * 2 + c];
  __syncthreads();
  dft8<-1>(a);
#pragma unroll
  for (int m = 1; m < 8; ++m) a[m] = cmul(a[m], tw[(8 * u0 * m) & 2047]);
#pragma unroll
  for (int m = 0; m < 8; ++m) ex[((m1 * 8 + m) * 36 + u0) * 2 + c] = a[m];
  __syncthreads();
#pragma unroll
  for (int j = 0; j < 8; ++j) a[j] = ex[(g * 36 + v0 + 4 * j) * 2 + c];
  __syncthreads();
  dft8<-1>(a);
#pragma unroll
  for (int m = 1; m < 8; ++m) a[m] = cmul(a[m], tw[(64 * v0 * m) & 2047]);
#pragma unroll
  for (int m = 0; m < 8; ++m) ex[g * 72 + (m * 4 + v0) * 2 + c] = a[m];
  __syncthreads();
#pragma unroll
  for (int e = 0; e < 2; ++e)
#pragma unroll
    for (int v = 0; v < 4; ++v) a[e * 4 + v] = ex[g * 72 + ((2 * v0 + e) * 4 + v) * 2 + c];
  __syncthreads();
  dft4<-1>(a[0], a[1], a[2], a[3]);
  dft4<-1>(a[4], a[5], a[6], a[7]);
  if (kp == 0) {
    if (c == 0) {
#pragma unroll
      for (int i = 0; i < 8; ++i) ex[k2_freq(i, u)] = a[i];
    }
    __syncthreads();
    if (c == 0) {
#pragma unroll
      for (int i = 0; i < 8; ++i) {
        const int f = k2_freq(i, u), fm = (2048 - f) & 2047;
        const float2 pm = ex[fm];
        const float w0r = bf2f(wr[(size_t)f * DF_]), w0i = bf2f(wi[(size_t)f * DF_]), w0mr = bf2f(wr[(size_t)fm * DF_]), w0mi = bf2f(wi[(size_t)fm * DF_]);
        const float wnr = bf2f(wr[(size_t)f * DF_ + 512]), wni = bf2f(wi[(size_t)f * DF_ + 512]), wnmr = bf2f(wr[(size_t)fm * DF_ + 512]), wnmi = bf2f(wi[(size_t)fm * DF_ + 512]);
        const float2 A = make_float2(0.5f * (w0r + w0mr), 0.5f * (w0i - w0mi));
        const float2 B = make_float2(0.5f * (wnr + wnmr), 0.5f * (wni - wnmi));
        const float2 C1 = make_float2(0.5f * (A.x + B.x), 0.5f * (A.y + B.y)), C2 = make_float2(0.5f * (A.x - B.x), 0.5f * (A.y - B.y));
        a[i] = cadd(cmul(a[i], C1), cmul(make_float2(pm.x, -pm.y), C2));
      }
    } else {
#pragma unroll
      for (int i = 0; i < 8; ++i) a[i] = cmul(a[i], make_float2(__uint_as_float(wv[i] << 16), __uint_as_float(wv[i] & 0xffff0000u)));
    }
    __syncthreads();
  } else {
#pragma unroll
    for (int i = 0; i < 8; ++i) a[i] = cmul(a[i], make_float2(__uint_as_float(wv[i] << 16), __uint_as_float(wv[i] & 0xffff0000u)));
  }
  dft4<1>(a[0], a[1], a[2], a[3]);
  dft4<1>(a[4], a[5], a[6], a[7]);
#pragma unroll
  for (int e = 0; e < 2; ++e)
#pragma unroll
    for (int v = 1; v < 4; ++v) a[e * 4 + v] = cmulc(a[e * 4 + v], tw[(64 * v * (2 * v0 + e)) & 2047]);
#pragma unroll
  for (int e = 0; e < 2; ++e)
#pragma unroll
    for (int v = 0; v < 4; ++v) ex[g * 72 + ((2 * v0 + e) * 4 + v) * 2 + c] = a[e * 4 + v];
  __syncthreads();
#pragma unroll
  for (int m = 0; m < 8; ++m) a[m] = ex[g * 72 + (m * 4 + v0) * 2 + c];
  __syncthreads();
  dft8<1>(a);
  {
    const int m2 = g & 7;
#pragma unroll
    for (int j = 0; j < 8; ++j) a[j] = cmulc(a[j], tw[(8 * (v0 + 4 * j) * m2) & 2047]);
  }
#pragma unroll
  for (int j = 0; j < 8; ++j) ex[(g * 36 + v0 + 4 * j) * 2 + c] = a[j];
  __syncthreads();
#pragma unroll
  for (int m = 0; m < 8; ++m) a[m] = ex[((m1 * 8 + m) * 36 + u0) * 2 + c];
  __syncthreads();
  dft8<1>(a);
#pragma unroll
  for (int j = 0; j < 8; ++j) a[j] = cmulc(a[j], tw[((u0 + 32 * j) * m1) & 2047]);
#pragma unroll
  for (int j = 0; j < 8; ++j) ex[(m1 * 256 + u0 + 32 * j) * 2 + c] = a[j];
  __syncthreads();
#pragma unroll
  for (int m = 0; m < 8; ++m) a[m] = ex[(m * 256 + u) * 2 + c];
  dft8<1>(a);
#pragma unroll
  for (int j = 0; j < 8; ++j) col[(u + 256 * j) * 2 + c] = a[j];
}

__global__ __launch_bounds__(512) void k3_irfft_ln(const float2* __restrict__ T, const u16* __restrict__ x, u16* __restrict__ hout, u16* __restrict__ ln2) {
  __shared__ __attribute__((aligned(16))) float2 tw[1024];
  __shared__ __attribute__((aligned(16))) float2 buf[8 * RS_];
  const int tid = threadIdx.x, lane = tid & 63, w = tid >> 6, blk = blockIdx.x;
  const int row = blk * 8 + w;
  {
    const int r = tid & 7;
#pragma unroll
    for (int it = 0; it < 4; ++it) {
      const int kp = (tid >> 3) + 64 * it;
      const float4 v = *(const float4*)(T + ((size_t)kp * S_ + blk * 8 + r) * 2);
      *(float4*)(buf + r * RS_ + 2 * kp) = v;
    }
  }
  const u32* xr = (const u32*)(x + (size_t)row * D_);
  u32 raw[8];
#pragma unroll
  for (int j = 0; j < 8; ++j) raw[j] = xr[lane + 64 * j];
  for (int j = tid; j < 1024; j += 512) { float s, c; sincospif(-(float)j * (1.0f / 512.0f), &s, &c); tw[j] = make_float2(c, s); }
  __syncthreads();
  float2* bw = buf + w * RS_;
  float2 a[8];
#pragma unroll
  for (int j = 0; j < 8; ++j) {
    const int k = lane + 64 * j;
    const float2 yk = bw[k], ym = bw[(512 - k) & 511];
    const float2 sum2 = make_float2(yk.x + ym.x, yk.y - ym.y);
    const float2 dif2 = make_float2(yk.x - ym.x, yk.y + ym.y);
    const float2 t = cmulc(dif2, tw[k]);
    a[j] = make_float2(sum2.x - t.y, sum2.y + t.x);
    if (k == 0) a[j] = make_float2(yk.x + yk.y, yk.x - yk.y);
  }
  __builtin_amdgcn_s_waitcnt(0xC07F);
  __builtin_amdgcn_wave_barrier();
  fft512_wave<1>(a, bw, tw, lane);
  const float scale = 1.0f / 4194304.0f;
  float sum = 0.f;
#pragma unroll
  for (int j = 0; j < 8; ++j) {
    a[j].x = bfr(__uint_as_float(raw[j] << 16) + bfr(a[j].x * scale));
    a[j].y = bfr(__uint_as_float(raw[j] & 0xffff0000u) + bfr(a[j].y * scale));
    sum += a[j].x + a[j].y;
  }
  const float mean = wave_sum(sum) * (1.0f / 1024.0f);
  float sq = 0.f;
#pragma unroll
  for (int j = 0; j < 8; ++j) { const float dx = a[j].x - mean, dy = a[j].y - mean; sq += dx * dx + dy * dy; }
  const float var = wave_sum(sq) * (1.0f / 1024.0f);
  const float mb = bfr(mean), den = bfr(sqrtf(bfr(bfr(var) + 1e-5f)));
  u32* ho = (u32*)(hout + (size_t)row * D_);
  u32* lo = (u32*)(ln2 + (size_t)row * D_);
#pragma unroll
  for (int j = 0; j < 8; ++j) {
    ho[lane + 64 * j] = pk2(a[j].x, a[j].y);
    lo[lane + 64 * j] = pk2(bfr(a[j].x - mb) / den, bfr(a[j].y - mb) / den);
  }
}

#define WAIT_VM(n) asm volatile("s_waitcnt vmcnt(%0)" ::"n"(n) : "memory")
#define BARRIER() do { asm volatile("s_waitcnt lgkmcnt(0)" ::: "memory"); __builtin_amdgcn_s_barrier(); asm volatile("" ::: "memory"); } while (0)

__device__ __forceinline__ bf16x8 lds_a(const char* p) { return *(const bf16x8*)p; }
__device__ __forceinline__ bf16x8 lds_tr(const char* p0, const char* p1) {
  s16x4 lo = __builtin_amdgcn_ds_read_tr16_b64_v4i16((LDS_AS s16x4*)p0);
  s16x4 hi = __builtin_amdgcn_ds_read_tr16_b64_v4i16((LDS_AS s16x4*)p1);
  s16x8 v = __builtin_shufflevector(lo, hi, 0, 1, 2, 3, 4, 5, 6, 7);
  return __builtin_bit_cast(bf16x8, v);
}
__device__ __forceinline__ float mish_f(float h) {
  const float u = __expf(fminf(h, 30.f));
  const float n = u * (u + 2.f);
  return h * n * __builtin_amdgcn_rcpf(n + 2.f);
}

constexpr int G1_STAGE = 16384 + 28672, G1_NST = 3;
__global__ __launch_bounds__(256) void k4_gemm1(const u16* __restrict__ A, const u16* __restrict__ B, const u16* __restrict__ bias, u16* __restrict__ C) {
  extern __shared__ __attribute__((aligned(16))) char sm[];
  const int tid = threadIdx.x, lane = tid & 63, w = __builtin_amdgcn_readfirstlane(tid >> 6);
  const int wr = w >> 1, wc = w & 1, fr = lane & 15, fq = lane >> 4;
  const int xcd = blockIdx.x & 7, idx = blockIdx.x >> 3;
  const int tm = (xcd & 1) * 8 + (idx & 7), tn = (xcd >> 1) * 4 + (idx >> 3);
  const int m0 = tm * 128, n0 = tn * 224;
  auto rsA = __builtin_amdgcn_make_buffer_rsrc((void*)A, 0, S_ * D_ * 2, 0x00020000);
  auto rsB = __builtin_amdgcn_make_buffer_rsrc((void*)B, 0, D_ * F_ * 2, 0x00020000);
  int voA[4], voB[7];
#pragma unroll
  for (int i = 0; i < 4; ++i) { const int row = (w * 4 + i) * 8 + (lane >> 3), cch = (lane & 7) ^ (row & 7); voA[i] = (m0 + row) * (D_ * 2) + cch * 16; }
#pragma unroll
  for (int i = 0; i < 7; ++i) {
    const int L = (w * 7 + i) * 64 + lane, k = L / 28, cc = L % 28, nb = (cc >> 1) ^ ((k >> 3) & 1);
    voB[i] = k * (F_ * 2) + (n0 + nb * 16 + (cc & 1) * 8) * 2;
  }
#define G1_STAGE_LOAD(st, kt) do { char* sb_ = sm + (st) * G1_STAGE; \
    _Pragma("unroll") for (int i = 0; i < 4; ++i) __builtin_amdgcn_raw_ptr_buffer_load_lds(rsA, (LDS_AS void*)(sb_ + (w * 4 + i) * 1024), 16, voA[i], (kt) * 128, 0, 0); \
    _Pragma("unroll") for (int i = 0; i < 7; ++i) __builtin_amdgcn_raw_ptr_buffer_load_lds(rsB, (LDS_AS void*)(sb_ + 16384 + (w * 7 + i) * 1024), 16, voB[i], (kt) * (64 * F_ * 2), 0, 0); } while (0)
  f32x4 acc[4][7];
#pragma unroll
  for (int i = 0; i < 4; ++i)
#pragma unroll
    for (int j = 0; j < 7; ++j) acc[i][j] = (f32x4){0.f, 0.f, 0.f, 0.f};
  const int aoff = (wr * 64 + fr) * 128;
  const int ach0 = ((0 + fq) ^ (fr & 7)) * 16, ach1 = ((4 + fq) ^ (fr & 7)) * 16;
  const int q = (lane & 15) >> 2, p = lane & 3;
  const int boff = 16384 + (8 * fq + q) * 448 + p * 8;
  const int gb = fq & 1;
  constexpr int NT = D_ / 64;
  G1_STAGE_LOAD(0, 0);
  G1_STAGE_LOAD(1, 1);
  int st = 0;
  for (int kt = 0; kt < NT; ++kt) {
    if (kt + 1 < NT) WAIT_VM(11); else WAIT_VM(0);
    BARRIER();
    if (kt + 2 < NT) { const int s2 = st == 0 ? 2 : st - 1; G1_STAGE_LOAD(s2, kt + 2); }
    const char* sb = sm + st * G1_STAGE;
#pragma unroll
    for (int ks = 0; ks < 2; ++ks) {
      bf16x8 af[4], bfm[7];
#pragma unroll
      for (int mi = 0; mi < 4; ++mi) af[mi] = lds_a(sb + aoff + mi * 2048 + (ks ? ach1 : ach0));
#pragma unroll
      for (int ni = 0; ni < 7; ++ni) {
        const int nb = wc * 7 + ni;
        const char* bp = sb + boff + ks * (32 * 448) + ((nb ^ gb) * 32);
        bfm[ni] = lds_tr(bp, bp + 4 * 448);
      }
#pragma unroll
      for (int mi = 0; mi < 4; ++mi)
#pragma unroll
        for (int ni = 0; ni < 7; ++ni) acc[mi][ni] = __builtin_amdgcn_mfma_f32_16x16x32_bf16(bfm[ni], af[mi], acc[mi][ni], 0, 0, 0);
    }
    st = st == 2 ? 0 : st + 1;
  }
#pragma unroll
  for (int ni = 0; ni < 7; ++ni) {
    const int n = n0 + wc * 112 + ni * 16 + fq * 4;
    const u32x2 bb = *(const u32x2*)(bias + n);
    const float b0 = __uint_as_float(bb.x << 16), b1 = __uint_as_float(bb.x & 0xffff0000u), b2 = __uint_as_float(bb.y << 16), b3 = __uint_as_float(bb.y & 0xffff0000u);
#pragma unroll
    for (int mi = 0; mi < 4; ++mi) {
      const int m = m0 + wr * 64 + mi * 16 + fr;
      const float h0 = bfr(bfr(acc[mi][ni][0]) + b0), h1 = bfr(bfr(acc[mi][ni][1]) + b1), h2 = bfr(bfr(acc[mi][ni][2]) + b2), h3 = bfr(bfr(acc[mi][ni][3]) + b3);
      u32x2 o; o.x = pk2(mish_f(h0), mish_f(h1)); o.y = pk2(mish_f(h2), mish_f(h3));
      *(u32x2*)(C + (size_t)m * F_ + n) = o;
    }
  }
}

constexpr int G2_STAGE = 32768 + 16384, G2_NST = 3;
__global__ __launch_bounds__(256) void k5_gemm2(const u16* __restrict__ A, const u16* __restrict__ B, const u16* __restrict__ H, u16* __restrict__ C) {
  extern __shared__ __attribute__((aligned(16))) char sm[];
  const int tid = threadIdx.x, lane = tid & 63, w = __builtin_amdgcn_readfirstlane(tid >> 6);
  const int fr = lane & 15, fq = lane >> 4;
  const int xcd = blockIdx.x & 7, idx = blockIdx.x >> 3;
  const int tm = (xcd >> 1) * 4 + (idx & 3), tn = (xcd & 1) * 8 + (idx >> 2);
  const int m0 = tm * 128, n0 = tn * 64;
  auto rsA = __builtin_amdgcn_make_buffer_rsrc((void*)A, 0, S_ * F_ * 2, 0x00020000);
  auto rsB = __builtin_amdgcn_make_buffer_rsrc((void*)B, 0, F_ * D_ * 2, 0x00020000);
  int voA[8], voB[4];
#pragma unroll
  for (int i = 0; i < 8; ++i) { const int pc = w * 8 + i, sub = pc >> 4, row = (pc & 15) * 8 + (lane >> 3), cch = (lane & 7) ^ (row & 7); voA[i] = (m0 + row) * (F_ * 2) + sub * 128 + cch * 16; }
#pragma unroll
  for (int i = 0; i < 4; ++i) {
    const int L = (w * 4 + i) * 64 + lane, k = L >> 3, cc = L & 7, f = ((k >> 1) & 1) | (((k >> 3) & 1) << 1), nb = (cc >> 1) ^ f;
    voB[i] = k * (D_ * 2) + (n0 + nb * 16 + (cc & 1) * 8) * 2;
  }
#define G2_STAGE_LOAD(st, kt) do { char* sb_ = sm + (st) * G2_STAGE; \
    _Pragma("unroll") for (int i = 0; i < 8; ++i) __builtin_amdgcn_raw_ptr_buffer_load_lds(rsA, (LDS_AS void*)(sb_ + (w * 8 + i) * 1024), 16, voA[i], (kt) * 256, 0, 0); \
    _Pragma("unroll") for (int i = 0; i < 4; ++i) __builtin_amdgcn_raw_ptr_buffer_load_lds(rsB, (LDS_AS void*)(sb_ + 32768 + (w * 4 + i) * 1024), 16, voB[i], (kt) * (128 * D_ * 2), 0, 0); } while (0)
  f32x4 acc[8][4];
#pragma unroll
  for (int i = 0; i < 8; ++i)
#pragma unroll
    for (int j = 0; j < 4; ++j) acc[i][j] = (f32x4){0.f, 0.f, 0.f, 0.f};
  const int aoff = (w >> 1) * 16384 + fr * 128 + ((((w & 1) * 4 + fq) ^ (fr & 7)) * 16);
  const int q = (lane & 15) >> 2, p = lane & 3;
  const int bfx = ((q >> 1) & 1) | ((fq & 1) << 1);
  const int boff = 32768 + (32 * w + 8 * fq + q) * 128 + p * 8;
  constexpr int NT = F_ / 128;
  G2_STAGE_LOAD(0, 0);
  G2_STAGE_LOAD(1, 1);
  int st = 0;
  for (int kt = 0; kt < NT; ++kt) {
    if (kt + 1 < NT) WAIT_VM(12); else WAIT_VM(0);
    BARRIER();
    if (kt + 2 < NT) { const int s2 = st == 0 ? 2 : st - 1; G2_STAGE_LOAD(s2, kt + 2); }
    const char* sb = sm + st * G2_STAGE;
    bf16x8 af[8], bfm[4];
#pragma unroll
    for (int ni = 0; ni < 4; ++ni) { const char* bp = sb + boff + ((ni ^ bfx) * 32); bfm[ni] = lds_tr(bp, bp + 512); }
#pragma unroll
    for (int mi = 0; mi < 8; ++mi) af[mi] = lds_a(sb + aoff + mi * 2048);
#pragma unroll
    for (int mi = 0; mi < 8; ++mi)
#pragma unroll
      for (int ni = 0; ni < 4; ++ni) acc[mi][ni] = __builtin_amdgcn_mfma_f32_16x16x32_bf16(bfm[ni], af[mi], acc[mi][ni], 0, 0, 0);
    st = st == 2 ? 0 : st + 1;
  }
  BARRIER();
  f32x4* red = (f32x4*)sm;
#pragma unroll
  for (int mi = 0; mi < 8; ++mi)
    if ((mi >> 1) != w) {
#pragma unroll
      for (int ni = 0; ni < 4; ++ni) red[((w * 8 + mi) * 4 + ni) * 64 + lane] = acc[mi][ni];
    }
  BARRIER();
#pragma unroll
  for (int e = 0; e < 2; ++e) {
#pragma unroll
    for (int ni = 0; ni < 4; ++ni) {
      f32x4 s = (f32x4){0.f, 0.f, 0.f, 0.f};
#pragma unroll
      for (int mi = 0; mi < 8; ++mi) if (mi == 2 * w + e) s = acc[mi][ni];
#pragma unroll
      for (int sw = 0; sw < 4; ++sw) if (sw != w) s += red[((sw * 8 + 2 * w + e) * 4 + ni) * 64 + lane];
      const int m = m0 + (2 * w + e) * 16 + fr, n = n0 + ni * 16 + fq * 4;
      const u32x2 hh = *(const u32x2*)(H + (size_t)m * D_ + n);
      u32x2 o;
      o.x = pk2(__uint_as_float(hh.x << 16) + bfr(s[0]), __uint_as_float(hh.x & 0xffff0000u) + bfr(s[1]));
      o.y = pk2(__uint_as_float(hh.y << 16) + bfr(s[2]), __uint_as_float(hh.y & 0xffff0000u) + bfr(s[3]));
      *(u32x2*)(C + (size_t)m * D_ + n) = o;
    }
  }
}

extern "C" void kernel_launch(void* const* d_in, const int* in_sizes, int n_in, void* d_out, int out_size, void* d_ws, size_t ws_size,
                              hipStream_t stream) {
  const u16* x = (const u16*)d_in[0];
  const u16* wr = (const u16*)d_in[1];
  const u16* wi = (const u16*)d_in[2];
  const u16* w1 = (const u16*)d_in[4];
  const u16* b1 = (const u16*)d_in[5];
  const u16* w2 = (const u16*)d_in[6];
  char* ws = (char*)d_ws;
  float2* T = (float2*)ws;
  u32* Wt = (u32*)(ws + (8u << 20));
  u16* h = (u16*)(ws + (12u << 20));
  u16* ln2 = (u16*)(ws + (16u << 20));
  u16* act = (u16*)(ws + (20u << 20));
  (void)hipFuncSetAttribute((const void*)k4_gemm1, hipFuncAttributeMaxDynamicSharedMemorySize, G1_STAGE * G1_NST);
  (void)hipFuncSetAttribute((const void*)k5_gemm2, hipFuncAttributeMaxDynamicSharedMemorySize, G2_STAGE * G2_NST);
  k1_ln_rfft<<<256, 512, 0, stream>>>(x, wr, wi, T, Wt);
  k2_col<<<256, 512, 0, stream>>>(T, Wt, wr, wi);
  k3_irfft_ln<<<256, 512, 0, stream>>>(T, x, h, ln2);
  k4_gemm1<<<256, 256, G1_STAGE * G1_NST, stream>>>(ln2, w1, b1, act);
  k5_gemm2<<<256, 256, G2_STAGE * G2_NST, stream>>>(act, w2, h, (u16*)d_out);
}
```

```cpp
#include <hip/hip_runtime.h>
#include <hip/hip_bf16.h>

typedef unsigned short u16;
typedef unsigned int u32;
typedef short s16x4 __attribute__((ext_vector_type(4)));
typedef short s16x8 __attribute__((ext_vector_type(8)));
typedef __bf16 bf16x8 __attribute__((ext_vector_type(8)));
typedef float f32x4 __attribute__((ext_vector_type(4)));
typedef u32 u32x2 __attribute__((ext_vector_type(2)));
#define LDS_AS __attribute__((address_space(3)))

#define STAMP(k, i)
constexpr int S_ = 2048, D_ = 1024, F_ = 3584, DF_ = 513;

__device__ __forceinline__ float bf2f(u16 v) { return __uint_as_float(((u32)v) << 16); }
__device__ __forceinline__ u16 f2bf(float f) { return __builtin_bit_cast(u16, (__bf16)f); }
__device__ __forceinline__ float bfr(float f) { return bf2f(f2bf(f)); }
__device__ __forceinline__ u32 pk2(float lo, float hi) { return (u32)f2bf(lo) | ((u32)f2bf(hi) << 16); }

__device__ __forceinline__ float2 cadd(float2 a, float2 b) { return make_float2(a.x + b.x, a.y + b.y); }
__device__ __forceinline__ float2 csub(float2 a, float2 b) { return make_float2(a.x - b.x, a.y - b.y); }
__device__ __forceinline__ float2 cmul(float2 a, float2 b) { return make_float2(a.x * b.x - a.y * b.y, a.x * b.y + a.y * b.x); }
__device__ __forceinline__ float2 cmulc(float2 a, float2 b) { return make_float2(a.x * b.x + a.y * b.y, a.y * b.x - a.x * b.y); }
template <int SGN> __device__ __forceinline__ float2 twm(float2 a, float2 w) { return SGN < 0 ? cmul(a, w) : cmulc(a, w); }
template <int SGN> __device__ __forceinline__ float2 muli(float2 a) { return SGN < 0 ? make_float2(a.y, -a.x) : make_float2(-a.y, a.x); }

template <int SGN> __device__ __forceinline__ void dft4(float2& b0, float2& b1, float2& b2, float2& b3) {
  float2 s02 = cadd(b0, b2), d02 = csub(b0, b2), s13 = cadd(b1, b3), d13 = muli<SGN>(csub(b1, b3));
  b0 = cadd(s02, s13); b2 = csub(s02, s13); b1 = cadd(d02, d13); b3 = csub(d02, d13);
}
template <int SGN> __device__ __forceinline__ void dft8(float2 (&a)[8]) {
  const float R = 0.70710678118654752440f;
  float2 e0 = cadd(a[0], a[4]), e1 = cadd(a[1], a[5]), e2 = cadd(a[2], a[6]), e3 = cadd(a[3], a[7]);
  float2 o0 = csub(a[0], a[4]), o1 = csub(a[1], a[5]), o2 = csub(a[2], a[6]), o3 = csub(a[3], a[7]);
  if (SGN < 0) {
    o1 = make_float2((o1.x + o1.y) * R, (o1.y - o1.x) * R);
    o3 = make_float2((o3.y - o3.x) * R, -(o3.x + o3.y) * R);
  } else {
    o1 = make_float2((o1.x - o1.y) * R, (o1.x + o1.y) * R);
    o3 = make_float2(-(o3.x + o3.y) * R, (o3.x - o3.y) * R);
  }
  o2 = muli<SGN>(o2);
  dft4<SGN>(e0, e1, e2, e3);
  dft4<SGN>(o0, o1, o2, o3);
  a[0] = e0; a[2] = e1; a[4] = e2; a[6] = e3;
  a[1] = o0; a[3] = o1; a[5] = o2; a[7] = o3;
}

__device__ __forceinline__ float wave_sum(float v) {
  v += __builtin_bit_cast(float, __builtin_amdgcn_update_dpp(0, __builtin_bit_cast(int, v), 0x111, 0xf, 0xf, true));
  v += __builtin_bit_cast(float, __builtin_amdgcn_update_dpp(0, __builtin_bit_cast(int, v), 0x112, 0xf, 0xf, true));
  v += __builtin_bit_cast(float, __builtin_amdgcn_update_dpp(0, __builtin_bit_cast(int, v), 0x114, 0xf, 0xf, true));
  v += __builtin_bit_cast(float, __builtin_amdgcn_update_dpp(0, __builtin_bit_cast(int, v), 0x118, 0xf, 0xf, true));
  v += __builtin_bit_cast(float, __builtin_amdgcn_update_dpp(0, __builtin_bit_cast(int, v), 0x142, 0xa, 0xf, true));
  v += __builtin_bit_cast(float, __builtin_amdgcn_update_dpp(0, __builtin_bit_cast(int, v), 0x143, 0xc, 0xf, true));
  return __builtin_bit_cast(float, __builtin_amdgcn_readlane(__builtin_bit_cast(int, v), 63));
}
__device__ __forceinline__ float2 cispi_neg(float x) { float s, c; sincospif(x, &s, &c); return make_float2(c, -s); }
__device__ __forceinline__ void pow7(float2 w, float2 (&p)[8]) {
  p[0] = make_float2(1.f, 0.f);
  p[1] = w; p[2] = cmul(w, w); p[3] = cmul(p[2], w); p[4] = cmul(p[2], p[2]);
  p[5] = cmul(p[4], w); p[6] = cmul(p[3], p[3]); p[7] = cmul(p[4], p[3]);
}
#define LGKM0() do { __builtin_amdgcn_s_waitcnt(0xC07F); __builtin_amdgcn_wave_barrier(); } while (0)

template <int SGN> __device__ __forceinline__ void fft512_wave(float2 (&a)[8], float2* bw, int lane) {
  const int l0 = lane & 7, mm = lane >> 3;
  float2 p1[8], p2[8];
  pow7(cispi_neg((float)lane * (1.0f / 256.0f)), p1);
  pow7(cispi_neg((float)l0 * (1.0f / 32.0f)), p2);
  dft8<SGN>(a);
#pragma unroll
  for (int m = 1; m < 8; ++m) a[m] = twm<SGN>(a[m], p1[m]);
#pragma unroll
  for (int m = 0; m < 8; ++m) bw[m * 72 + lane] = a[m];
  LGKM0();
#pragma unroll
  for (int j = 0; j < 8; ++j) a[j] = bw[mm * 72 + l0 + 8 * j];
  LGKM0();
  dft8<SGN>(a);
#pragma unroll
  for (int m = 1; m < 8; ++m) a[m] = twm<SGN>(a[m], p2[m]);
#pragma unroll
  for (int m = 0; m < 8; ++m) bw[l0 * 66 + mm + 8 * m] = a[m];
  LGKM0();
#pragma unroll
  for (int j = 0; j < 8; ++j) a[j] = bw[j * 66 + lane];
  LGKM0();
  dft8<SGN>(a);
}

constexpr int RS_ = 584;

__device__ __forceinline__ int k2_freq(int i, int tau) {
  const int h = tau & 3, m2 = (tau >> 2) & 7, m1 = tau >> 5, e = i >> 2, m4 = i & 3;
  return m1 + 8 * m2 + 64 * (2 * h + e) + 512 * m4;
}
__device__ __forceinline__ float2 rot8(float2 w, int it) {
  const float R = 0.70710678118654752440f;
  return it == 0 ? w : it == 1 ? make_float2((w.x + w.y) * R, (w.y - w.x) * R) : it == 2 ? make_float2(w.y, -w.x) : make_float2((w.y - w.x) * R, -(w.x + w.y) * R);
}

__global__ __launch_bounds__(512) void k1_ln_rfft(const u16* __restrict__ x, const u16* __restrict__ wr, const u16* __restrict__ wi,
                                                  float2* __restrict__ T, u32* __restrict__ Wt, float4* __restrict__ Cs) {
  __shared__ __attribute__((aligned(16))) float2 buf[8 * RS_];
  __shared__ u32 wsm[8][516];
  const int tid = threadIdx.x, lane = tid & 63, w = tid >> 6, blk = blockIdx.x;
  const int row = blk * 8 + w;
  STAMP(0, 0);
  const u32* xr = (const u32*)(x + (size_t)row * D_);
  u32 raw[8];
#pragma unroll
  for (int j = 0; j < 8; ++j) raw[j] = xr[lane + 64 * j];
  const int f = k2_freq(blk >> 5, ((blk & 31) << 3) + w);
  const u16* pr = wr + (size_t)f * DF_;
  const u16* pi = wi + (size_t)f * DF_;
  u16 wra[8], wia[8];
#pragma unroll
  for (int j = 0; j < 8; ++j) { wra[j] = pr[lane + 64 * j]; wia[j] = pi[lane + 64 * j]; }
  if (lane == 0) {
    const int fm = (2048 - f) & 2047;
    const float w0r = bf2f(pr[0]), w0i = bf2f(pi[0]), w0mr = bf2f(wr[(size_t)fm * DF_]), w0mi = bf2f(wi[(size_t)fm * DF_]);
    const float wnr = bf2f(pr[512]), wni = bf2f(pi[512]), wnmr = bf2f(wr[(size_t)fm * DF_ + 512]), wnmi = bf2f(wi[(size_t)fm * DF_ + 512]);
    const float2 A = make_float2(0.5f * (w0r + w0mr), 0.5f * (w0i - w0mi));
    const float2 B = make_float2(0.5f * (wnr + wnmr), 0.5f * (wni - wnmi));
    Cs[(blk >> 5) * 256 + ((blk & 31) << 3) + w] = make_float4(0.5f * (A.x + B.x), 0.5f * (A.y + B.y), 0.5f * (A.x - B.x), 0.5f * (A.y - B.y));
  }
  const float2 wpost = cispi_neg((float)(tid >> 3) * (1.0f / 256.0f));
  float2 a[8];
  float sum = 0.f;
#pragma unroll
  for (int j = 0; j < 8; ++j) { a[j] = make_float2(__uint_as_float(raw[j] << 16), __uint_as_float(raw[j] & 0xffff0000u)); sum += a[j].x + a[j].y; }
  const float mean = wave_sum(sum) * (1.0f / 1024.0f);
  float sq = 0.f;
#pragma unroll
  for (int j = 0; j < 8; ++j) { const float dx = a[j].x - mean, dy = a[j].y - mean; sq += dx * dx + dy * dy; }
  const float var = wave_sum(sq) * (1.0f / 1024.0f);
  const float mb = bfr(mean), rden = 1.0f / bfr(sqrtf(bfr(bfr(var) + 1e-5f)));
#pragma unroll
  for (int j = 0; j < 8; ++j) { a[j].x = bfr(bfr(a[j].x - mb) * rden); a[j].y = bfr(bfr(a[j].y - mb) * rden); }
  STAMP(0, 1);
  float2* bw = buf + w * RS_;
  fft512_wave<-1>(a, bw, lane);
#pragma unroll
  for (int m = 0; m < 8; ++m) bw[lane + 64 * m] = a[m];
#pragma unroll
  for (int j = 0; j < 8; ++j) wsm[w][lane + 64 * j] = (u32)wra[j] | ((u32)wia[j] << 16);
  __syncthreads();
  STAMP(0, 2);
  const int r = tid & 7;
  const float2* zr = buf + r * RS_;
  const float2 w1 = make_float2(0.99998117528260111f, -0.0061358846491544753f);
#pragma unroll
  for (int it = 0; it < 4; ++it) {
    const int kp = (tid >> 3) + 64 * it;
    const float2 we = rot8(wpost, it);
    float4 o;
#pragma unroll
    for (int e = 0; e < 2; ++e) {
      const int k = 2 * kp + e;
      const float2 zk = zr[k], zm = zr[(512 - k) & 511];
      const float2 sum2 = make_float2(zk.x + zm.x, zk.y - zm.y);
      const float2 dif2 = make_float2(zk.x - zm.x, zk.y + zm.y);
      const float2 t = cmul(dif2, e ? cmul(we, w1) : we);
      float2 X = make_float2(sum2.x + t.y, sum2.y - t.x);
      if (k == 0) X = make_float2(2.f * (zk.x + zk.y), 2.f * (zk.x - zk.y));
      if (e == 0) { o.x = X.x; o.y = X.y; } else { o.z = X.x; o.w = X.y; }
    }
    *(float4*)(T + ((size_t)kp * S_ + blk * 8 + r) * 2) = o;
  }
  {
    const int i = blk >> 5, tau0 = (blk & 31) << 3;
#pragma unroll
    for (int it = 0; it < 4; ++it) {
      const int kp = (tid >> 3) + 64 * it;
      u32x2 v; v.x = wsm[r][2 * kp]; v.y = wsm[r][2 * kp + 1];
      *(u32x2*)(Wt + ((size_t)kp * 8 + i) * 512 + 2 * (tau0 + r)) = v;
    }
  }
  STAMP(0, 3);
  STAMP(0, 4);
}

__device__ __forceinline__ int ex3(int g, int m, int v, int c) { return g * 74 + (m * 4 + v) * 2 + c + (m >> 2) * 8; }
__global__ __launch_bounds__(512) void k2_col(float2* __restrict__ T, const u32* __restrict__ Wt, const float4* __restrict__ Cs) {
  __shared__ __attribute__((aligned(16))) float2 exA[4736];
  __shared__ __attribute__((aligned(16))) float2 exB[4736];
  const int t = threadIdx.x, kp = blockIdx.x, c = t & 1, u = t >> 1;
  float2* col = T + (size_t)kp * S_ * 2;
  STAMP(1, 0);
  float2 a[8];
#pragma unroll
  for (int j = 0; j < 8; ++j) a[j] = col[(u + 256 * j) * 2 + c];
  u32 wv[8];
#pragma unroll
  for (int i = 0; i < 8; ++i) wv[i] = Wt[((size_t)kp * 8 + i) * 512 + t];
  const int u0 = u & 31, m1 = u >> 5;
  const int v0 = u & 3, g = u >> 2;
  float2 p1[8], p2[8], p3[8];
  pow7(cispi_neg((float)u * (1.0f / 1024.0f)), p1);
  pow7(cispi_neg((float)u0 * (1.0f / 128.0f)), p2);
  pow7(cispi_neg((float)v0 * (1.0f / 16.0f)), p3);
  STAMP(1, 1);
  dft8<-1>(a);
#pragma unroll
  for (int m = 1; m < 8; ++m) a[m] = cmul(a[m], p1[m]);
#pragma unroll
  for (int m = 0; m < 8; ++m) exA[(m * 256 + u) * 2 + c] = a[m];
  __syncthreads();
#pragma unroll
  for (int j = 0; j < 8; ++j) a[j] = exA[(m1 * 256 + u0 + 32 * j) * 2 + c];
  dft8<-1>(a);
#pragma unroll
  for (int m = 1; m < 8; ++m) a[m] = cmul(a[m], p2[m]);
#pragma unroll
  for (int m = 0; m < 8; ++m) exB[((m1 * 8 + m) * 36 + u0) * 2 + c] = a[m];
  __syncthreads();
#pragma unroll
  for (int j = 0; j < 8; ++j) a[j] = exB[(g * 36 + v0 + 4 * j) * 2 + c];
  dft8<-1>(a);
#pragma unroll
  for (int m = 1; m < 8; ++m) a[m] = cmul(a[m], p3[m]);
#pragma unroll
  for (int m = 0; m < 8; ++m) exA[ex3(g, m, v0, c)] = a[m];
  __syncthreads();
#pragma unroll
  for (int e = 0; e < 2; ++e)
#pragma unroll
    for (int v = 0; v < 4; ++v) a[e * 4 + v] = exA[ex3(g, 2 * v0 + e, v, c)];
  dft4<-1>(a[0], a[1], a[2], a[3]);
  dft4<-1>(a[4], a[5], a[6], a[7]);
  STAMP(1, 2);
  if (kp == 0) {
    if (c == 0) {
#pragma unroll
      for (int i = 0; i < 8; ++i) exB[k2_freq(i, u)] = a[i];
    }
    __syncthreads();
    if (c == 0) {
#pragma unroll
      for (int i = 0; i < 8; ++i) {
        const int f = k2_freq(i, u), fm = (2048 - f) & 2047;
        const float2 pm = exB[fm];
        const float4 cs = Cs[i * 256 + u];
        const float2 C1 = make_float2(cs.x, cs.y), C2 = make_float2(cs.z, cs.w);
        a[i] = cadd(cmul(a[i], C1), cmul(make_float2(pm.x, -pm.y), C2));
      }
    } else {
#pragma unroll
      for (int i = 0; i < 8; ++i) a[i] = cmul(a[i], make_float2(__uint_as_float(wv[i] << 16), __uint_as_float(wv[i] & 0xffff0000u)));
    }
    __syncthreads();
  } else {
#pragma unroll
    for (int i = 0; i < 8; ++i) a[i] = cmul(a[i], make_float2(__uint_as_float(wv[i] << 16), __uint_as_float(wv[i] & 0xffff0000u)));
  }
  dft4<1>(a[0], a[1], a[2], a[3]);
  dft4<1>(a[4], a[5], a[6], a[7]);
#pragma unroll
  for (int e = 0; e < 2; ++e)
#pragma unroll
    for (int v = 0; v < 4; ++v) exB[ex3(g, 2 * v0 + e, v, c)] = a[e * 4 + v];
  __syncthreads();
#pragma unroll
  for (int m = 0; m < 8; ++m) a[m] = exB[ex3(g, m, v0, c)];
#pragma unroll
  for (int m = 1; m < 8; ++m) a[m] = cmulc(a[m], p3[m]);
  dft8<1>(a);
#pragma unroll
  for (int j = 0; j < 8; ++j) exA[(g * 36 + v0 + 4 * j) * 2 + c] = a[j];
  __syncthreads();
#pragma unroll
  for (int m = 0; m < 8; ++m) a[m] = exA[((m1 * 8 + m) * 36 + u0) * 2 + c];
#pragma unroll
  for (int m = 1; m < 8; ++m) a[m] = cmulc(a[m], p2[m]);
  dft8<1>(a);
#pragma unroll
  for (int j = 0; j < 8; ++j) exB[(m1 * 256 + u0 + 32 * j) * 2 + c] = a[j];
  __syncthreads();
#pragma unroll
  for (int m = 0; m < 8; ++m) a[m] = exB[(m * 256 + u) * 2 + c];
#pragma unroll
  for (int m = 1; m < 8; ++m) a[m] = cmulc(a[m], p1[m]);
  dft8<1>(a);
#pragma unroll
  for (int j = 0; j < 8; ++j) col[(u + 256 * j) * 2 + c] = a[j];
  STAMP(1, 3);
  STAMP(1, 4);
}

__global__ __launch_bounds__(512) void k3_irfft_ln(const float2* __restrict__ T, const u16* __restrict__ x, u16* __restrict__ hout, u16* __restrict__ ln2) {
  __shared__ __attribute__((aligned(16))) float2 buf[8 * RS_];
  const int tid = threadIdx.x, lane = tid & 63, w = tid >> 6, blk = blockIdx.x;
  const int row = blk * 8 + w;
  STAMP(2, 0);
  {
    const int r = tid & 7;
    float4 v[4];
#pragma unroll
    for (int it = 0; it < 4; ++it) v[it] = *(const float4*)(T + ((size_t)((tid >> 3) + 64 * it) * S_ + blk * 8 + r) * 2);
#pragma unroll
    for (int it = 0; it < 4; ++it) *(float4*)(buf + r * RS_ + 2 * ((tid >> 3) + 64 * it)) = v[it];
  }
  const u32* xr = (const u32*)(x + (size_t)row * D_);
  u32 raw[8];
#pragma unroll
  for (int j = 0; j < 8; ++j) raw[j] = xr[lane + 64 * j];
  const float2 wl = cispi_neg((float)lane * (1.0f / 512.0f));
  __syncthreads();
  STAMP(2, 1);
  float2* bw = buf + w * RS_;
  float2 a[8];
  const float C8 = 0.92387953251128674f, S8 = 0.38268343236508977f;
  const float2 wl2 = cmul(wl, make_float2(C8, -S8));
#pragma unroll
  for (int j = 0; j < 8; ++j) {
    const int k = lane + 64 * j;
    const float2 yk = bw[k], ym = bw[(512 - k) & 511];
    const float2 sum2 = make_float2(yk.x + ym.x, yk.y - ym.y);
    const float2 dif2 = make_float2(yk.x - ym.x, yk.y + ym.y);
    const float2 t = cmulc(dif2, rot8((j & 1) ? wl2 : wl, j >> 1));
    a[j] = make_float2(sum2.x - t.y, sum2.y + t.x);
    if (k == 0) a[j] = make_float2(yk.x + yk.y, yk.x - yk.y);
  }
  LGKM0();
  fft512_wave<1>(a, bw, lane);
  STAMP(2, 2);
  const float scale = 1.0f / 4194304.0f;
  float sum = 0.f;
#pragma unroll
  for (int j = 0; j < 8; ++j) {
    a[j].x = bfr(__uint_as_float(raw[j] << 16) + bfr(a[j].x * scale));
    a[j].y = bfr(__uint_as_float(raw[j] & 0xffff0000u) + bfr(a[j].y * scale));
    sum += a[j].x + a[j].y;
  }
  u32* ho = (u32*)(hout + (size_t)row * D_);
  u32* lo = (u32*)(ln2 + (size_t)row * D_);
#pragma unroll
  for (int j = 0; j < 8; ++j) ho[lane + 64 * j] = pk2(a[j].x, a[j].y);
  const float mean = wave_sum(sum) * (1.0f / 1024.0f);
  float sq = 0.f;
#pragma unroll
  for (int j = 0; j < 8; ++j) { const float dx = a[j].x - mean, dy = a[j].y - mean; sq += dx * dx + dy * dy; }
  const float var = wave_sum(sq) * (1.0f / 1024.0f);
  const float mb = bfr(mean), rden = 1.0f / bfr(sqrtf(bfr(bfr(var) + 1e-5f)));
#pragma unroll
  for (int j = 0; j < 8; ++j) lo[lane + 64 * j] = pk2(bfr(a[j].x - mb) * rden, bfr(a[j].y - mb) * rden);
  STAMP(2, 3);
  STAMP(2, 4);
}

#define WAIT_VM(n) asm volatile("s_waitcnt vmcnt(%0)" ::"n"(n) : "memory")
#define BARRIER() do { asm volatile("s_waitcnt lgkmcnt(0)" ::: "memory"); __builtin_amdgcn_s_barrier(); asm volatile("" ::: "memory"); } while (0)

typedef int i32x4 __attribute__((ext_vector_type(4)));
__device__ __forceinline__ i32x4 mk_rsrc(const void* p, u32 bytes) {
  const unsigned long long a = (unsigned long long)p;
  i32x4 r; r.x = (int)(u32)a; r.y = (int)(u32)(a >> 32); r.z = (int)bytes; r.w = 0x00020000; return r;
}
__device__ __forceinline__ void dma16(i32x4 rs, u32 lds_addr, int voff, int soff) {
  asm volatile("s_mov_b32 m0, %0\n\ts_nop 0\n\tbuffer_load_dwordx4 %1, %2, %3 offen lds" ::"s"(lds_addr), "v"(voff), "s"(rs), "s"(soff) : "memory");
}
__device__ __forceinline__ bf16x8 lds_a(const char* p) { return *(const bf16x8*)p; }
__device__ __forceinline__ bf16x8 lds_tr(const char* p0, const char* p1) {
  s16x4 lo = __builtin_amdgcn_ds_read_tr16_b64_v4i16((LDS_AS s16x4*)p0);
  s16x4 hi = __builtin_amdgcn_ds_read_tr16_b64_v4i16((LDS_AS s16x4*)p1);
  s16x8 v = __builtin_shufflevector(lo, hi, 0, 1, 2, 3, 4, 5, 6, 7);
  return __builtin_bit_cast(bf16x8, v);
}
typedef float f32x2 __attribute__((ext_vector_type(2)));
__device__ __forceinline__ f32x2 mish2(f32x2 h) {
  f32x2 t; t.x = fminf(h.x, 30.f); t.y = fminf(h.y, 30.f);
  t = t * 1.4426950408889634f;
  f32x2 u; u.x = __builtin_amdgcn_exp2f(t.x); u.y = __builtin_amdgcn_exp2f(t.y);
  const f32x2 n = u * (u + 2.f), d = n + 2.f;
  f32x2 r; r.x = __builtin_amdgcn_rcpf(d.x); r.y = __builtin_amdgcn_rcpf(d.y);
  return (h * n) * r;
}

constexpr int G1_STAGE = 16384 + 28672, G1_NST = 3;
__global__ __launch_bounds__(512, 2) void k4_gemm1(const u16* __restrict__ A, const u16* __restrict__ B, const u16* __restrict__ bias, u16* __restrict__ C) {
  extern __shared__ __attribute__((aligned(16))) char sm[];
  const int tid = threadIdx.x, lane = tid & 63, wv = __builtin_amdgcn_readfirstlane(tid >> 6);
  const u32 smbase = (u32)(size_t)(LDS_AS char*)sm;
  const int xcd = blockIdx.x & 7, idx = blockIdx.x >> 3;
  const int tm = (xcd & 1) * 8 + (idx & 7), tn = (xcd >> 1) * 4 + (idx >> 3);
  const int m0 = tm * 128, n0 = tn * 224;
  STAMP(3, 0);
  constexpr int NT = D_ / 64;
  if (wv >= 4) {
    const int w = wv - 4;
    const i32x4 rsA = mk_rsrc(A, S_ * D_ * 2);
    const i32x4 rsB = mk_rsrc(B, D_ * F_ * 2);
    int voA[4], voB[7];
#pragma unroll
    for (int i = 0; i < 4; ++i) { const int row = (w * 4 + i) * 8 + (lane >> 3), cch = (lane & 7) ^ (row & 7); voA[i] = (m0 + row) * (D_ * 2) + cch * 16; }
#pragma unroll
    for (int i = 0; i < 7; ++i) {
      const int L = (w * 7 + i) * 64 + lane, k = L / 28, cc = L % 28, nb = (cc >> 1) ^ ((k >> 3) & 1);
      voB[i] = k * (F_ * 2) + (n0 + nb * 16 + (cc & 1) * 8) * 2;
    }
#define G1_STAGE_LOAD(st, kt) do { const u32 sb_ = smbase + (st) * G1_STAGE; \
    _Pragma("unroll") for (int i = 0; i < 4; ++i) dma16(rsA, sb_ + (w * 4 + i) * 1024, voA[i], (kt) * 128); \
    _Pragma("unroll") for (int i = 0; i < 7; ++i) dma16(rsB, sb_ + 16384 + (w * 7 + i) * 1024, voB[i], (kt) * (64 * F_ * 2)); } while (0)
    G1_STAGE_LOAD(0, 0);
    G1_STAGE_LOAD(1, 1);
    G1_STAGE_LOAD(2, 2);
    WAIT_VM(22);
    BARRIER();
    int st = 0;
    for (int kt = 0; kt < NT; ++kt) {
      if (kt + 2 < NT) WAIT_VM(11); else WAIT_VM(0);
      BARRIER();
      if (kt + 3 < NT) G1_STAGE_LOAD(st, kt + 3);
      st = st == 2 ? 0 : st + 1;
    }
  } else {
    const int wr = wv >> 1, wc = wv & 1, fr = lane & 15, fq = lane >> 4;
    f32x4 acc[4][7];
#pragma unroll
    for (int i = 0; i < 4; ++i)
#pragma unroll
      for (int j = 0; j < 7; ++j) acc[i][j] = (f32x4){0.f, 0.f, 0.f, 0.f};
    const int aoff = (wr * 64 + fr) * 128;
    const int ach0 = ((0 + fq) ^ (fr & 7)) * 16, ach1 = ((4 + fq) ^ (fr & 7)) * 16;
    const int q = (lane & 15) >> 2, p = lane & 3;
    const int boff = 16384 + (8 * fq + q) * 448 + p * 8;
    const int gb = fq & 1;
    bf16x8 ax[4], bx[7], ay[4], by[7];
#define G1_READ(AF, BF, sb, ks) do { \
    _Pragma("unroll") for (int mi = 0; mi < 4; ++mi) AF[mi] = lds_a((sb) + aoff + mi * 2048 + ((ks) ? ach1 : ach0)); \
    _Pragma("unroll") for (int ni = 0; ni < 7; ++ni) { const char* bp = (sb) + boff + (ks) * (32 * 448) + (((wc * 7 + ni) ^ gb) * 32); BF[ni] = lds_tr(bp, bp + 4 * 448); } } while (0)
#define G1_MMA(AF, BF) do { \
    _Pragma("unroll") for (int ni = 0; ni < 7; ++ni) _Pragma("unroll") for (int mi = 0; mi < 4; ++mi) \
      acc[mi][ni] = __builtin_amdgcn_mfma_f32_16x16x32_bf16(BF[ni], AF[mi], acc[mi][ni], 0, 0, 0); } while (0)
#define G1_SCHED() do { _Pragma("unroll") for (int i = 0; i < 18; ++i) { __builtin_amdgcn_sched_group_barrier(0x008, 1, 0); __builtin_amdgcn_sched_group_barrier(0x100, 1, 0); } \
    __builtin_amdgcn_sched_group_barrier(0x008, 10, 0); __builtin_amdgcn_sched_barrier(0); } while (0)
    BARRIER();
    G1_READ(ax, bx, sm, 0);
    STAMP(3, 1);
    int st = 0;
    for (int kt = 0; kt < NT; ++kt) {
      const char* sb = sm + st * G1_STAGE;
      const int sn = st == 2 ? 0 : st + 1;
      __builtin_amdgcn_sched_barrier(0);
      G1_READ(ay, by, sb, 1);
      G1_MMA(ax, bx);
      G1_SCHED();
      BARRIER();
      __builtin_amdgcn_sched_barrier(0);
      G1_READ(ax, bx, sm + sn * G1_STAGE, 0);
      G1_MMA(ay, by);
      G1_SCHED();
      st = sn;
    }
    STAMP(3, 2);
#pragma unroll
    for (int ni = 0; ni < 7; ++ni) {
      const int nl = wc * 112 + ni * 16 + fq * 4;
      const u32x2 bb = *(const u32x2*)(bias + n0 + nl);
      const float b0 = __uint_as_float(bb.x << 16), b1 = __uint_as_float(bb.x & 0xffff0000u), b2 = __uint_as_float(bb.y << 16), b3 = __uint_as_float(bb.y & 0xffff0000u);
#pragma unroll
      for (int mi = 0; mi < 4; ++mi) {
        const int ml = wr * 64 + mi * 16 + fr;
        const f32x2 r01 = mish2((f32x2){acc[mi][ni][0], acc[mi][ni][1]} + (f32x2){b0, b1}), r23 = mish2((f32x2){acc[mi][ni][2], acc[mi][ni][3]} + (f32x2){b2, b3});
        u32x2 o; o.x = pk2(r01.x, r01.y); o.y = pk2(r23.x, r23.y);
        *(u32x2*)(sm + ml * 464 + nl * 2) = o;
      }
    }
  }
  BARRIER();
  STAMP(3, 3);
#pragma unroll
  for (int i = 0; i < 7; ++i) {
    const int id = tid + 512 * i, row = id / 28, ch = id % 28;
    const uint4 v = *(const uint4*)(sm + row * 464 + ch * 16);
    *(uint4*)((char*)C + (size_t)(m0 + row) * (F_ * 2) + n0 * 2 + ch * 16) = v;
  }
  STAMP(3, 4);
}

constexpr int G2_STAGE = 32768 + 16384, G2_NST = 3;
__global__ __launch_bounds__(512, 2) void k5_gemm2(const u16* __restrict__ A, const u16* __restrict__ B, const u16* __restrict__ H, u16* __restrict__ C) {
  extern __shared__ __attribute__((aligned(16))) char sm[];
  const int tid = threadIdx.x, lane = tid & 63, wv = __builtin_amdgcn_readfirstlane(tid >> 6);
  const u32 smbase = (u32)(size_t)(LDS_AS char*)sm;
  const int xcd = blockIdx.x & 7, idx = blockIdx.x >> 3;
  const int tm = (xcd >> 1) * 4 + (idx & 3), tn = (xcd & 1) * 8 + (idx >> 2);
  const int m0 = tm * 128, n0 = tn * 64;
  STAMP(4, 0);
  constexpr int NT = F_ / 128;
  if (wv >= 4) {
    const int w = wv - 4;
    const i32x4 rsA = mk_rsrc(A, S_ * F_ * 2);
    const i32x4 rsB = mk_rsrc(B, F_ * D_ * 2);
    int voA[8], voB[4];
#pragma unroll
    for (int i = 0; i < 8; ++i) { const int pc = w * 8 + i, sub = pc >> 4, row = (pc & 15) * 8 + (lane >> 3), cch = (lane & 7) ^ (row & 7); voA[i] = (m0 + row) * (F_ * 2) + sub * 128 + cch * 16; }
#pragma unroll
    for (int i = 0; i < 4; ++i) {
      const int L = (w * 4 + i) * 64 + lane, k = L >> 3, cc = L & 7, f = ((k >> 1) & 1) | (((k >> 3) & 1) << 1), nb = (cc >> 1) ^ f;
      voB[i] = k * (D_ * 2) + (n0 + nb * 16 + (cc & 1) * 8) * 2;
    }
#define G2_STAGE_LOAD(st, kt) do { const u32 sb_ = smbase + (st) * G2_STAGE; \
    _Pragma("unroll") for (int i = 0; i < 8; ++i) dma16(rsA, sb_ + (w * 8 + i) * 1024, voA[i], (kt) * 256); \
    _Pragma("unroll") for (int i = 0; i < 4; ++i) dma16(rsB, sb_ + 32768 + (w * 4 + i) * 1024, voB[i], (kt) * (128 * D_ * 2)); } while (0)
    G2_STAGE_LOAD(0, 0);
    G2_STAGE_LOAD(1, 1);
    G2_STAGE_LOAD(2, 2);
    WAIT_VM(24);
    BARRIER();
    int st = 0;
    for (int kt = 0; kt < NT; ++kt) {
      if (kt + 2 < NT) WAIT_VM(12); else WAIT_VM(0);
      BARRIER();
      if (kt + 3 < NT) G2_STAGE_LOAD(st, kt + 3);
      st = st == 2 ? 0 : st + 1;
    }
    BARRIER();
  } else {
    const int w = wv, fr = lane & 15, fq = lane >> 4;
    f32x4 acc[8][4];
#pragma unroll
    for (int i = 0; i < 8; ++i)
#pragma unroll
      for (int j = 0; j < 4; ++j) acc[i][j] = (f32x4){0.f, 0.f, 0.f, 0.f};
    const int aoff = (w >> 1) * 16384 + fr * 128 + ((((w & 1) * 4 + fq) ^ (fr & 7)) * 16);
    const int q = (lane & 15) >> 2, p = lane & 3;
    const int bfx = ((q >> 1) & 1) | ((fq & 1) << 1);
    const int boff = 32768 + (32 * w + 8 * fq + q) * 128 + p * 8;
    bf16x8 ax[8], bx[4], ay[8], by[4];
#define G2_READ(AF, BF, sb) do { \
    _Pragma("unroll") for (int ni = 0; ni < 4; ++ni) { const char* bp = (sb) + boff + ((ni ^ bfx) * 32); BF[ni] = lds_tr(bp, bp + 512); } \
    _Pragma("unroll") for (int mi = 0; mi < 8; ++mi) AF[mi] = lds_a((sb) + aoff + mi * 2048); } while (0)
#define G2_MMA(AF, BF) do { \
    _Pragma("unroll") for (int mi = 0; mi < 8; ++mi) _Pragma("unroll") for (int ni = 0; ni < 4; ++ni) \
      acc[mi][ni] = __builtin_amdgcn_mfma_f32_16x16x32_bf16(BF[ni], AF[mi], acc[mi][ni], 0, 0, 0); } while (0)
#define G2_SCHED() do { _Pragma("unroll") for (int i = 0; i < 16; ++i) { __builtin_amdgcn_sched_group_barrier(0x008, 1, 0); __builtin_amdgcn_sched_group_barrier(0x100, 1, 0); } \
    __builtin_amdgcn_sched_group_barrier(0x008, 16, 0); __builtin_amdgcn_sched_barrier(0); } while (0)
#define G2_ITER(AC, BC, AN, BN) do { \
    const int sn = st == 2 ? 0 : st + 1; \
    BARRIER(); \
    __builtin_amdgcn_sched_barrier(0); \
    G2_READ(AN, BN, sm + sn * G2_STAGE); \
    G2_MMA(AC, BC); \
    G2_SCHED(); \
    st = sn; } while (0)
    BARRIER();
    G2_READ(ax, bx, sm);
    STAMP(4, 1);
    int st = 0;
    for (int kt = 0; kt < NT; kt += 2) {
      G2_ITER(ax, bx, ay, by);
      G2_ITER(ay, by, ax, bx);
    }
    STAMP(4, 2);
    BARRIER();
#pragma unroll
    for (int mi = 0; mi < 8; ++mi)
#pragma unroll
      for (int ni = 0; ni < 4; ++ni) *(f32x4*)(sm + w * 34816 + (mi * 16 + fr) * 272 + (ni * 16 + fq * 4) * 4) = acc[mi][ni];
  }
  uint4 hv[2];
#pragma unroll
  for (int i = 0; i < 2; ++i) { const int id = tid + 512 * i, row = id >> 3, ch = id & 7; hv[i] = *(const uint4*)(H + (size_t)(m0 + row) * D_ + n0 + ch * 8); }
  BARRIER();
  STAMP(4, 3);
#pragma unroll
  for (int i = 0; i < 2; ++i) {
    const int id = tid + 512 * i, row = id >> 3, ch = id & 7;
    f32x4 s0 = (f32x4){0.f, 0.f, 0.f, 0.f}, s1 = s0;
#pragma unroll
    for (int sw = 0; sw < 4; ++sw) { const char* pp = sm + sw * 34816 + row * 272 + ch * 32; s0 += *(const f32x4*)pp; s1 += *(const f32x4*)(pp + 16); }
    uint4 o;
    o.x = pk2(__uint_as_float(hv[i].x << 16) + bfr(s0[0]), __uint_as_float(hv[i].x & 0xffff0000u) + bfr(s0[1]));
    o.y = pk2(__uint_as_float(hv[i].y << 16) + bfr(s0[2]), __uint_as_float(hv[i].y & 0xffff0000u) + bfr(s0[3]));
    o.z = pk2(__uint_as_float(hv[i].z << 16) + bfr(s1[0]), __uint_as_float(hv[i].z & 0xffff0000u) + bfr(s1[1]));
    o.w = pk2(__uint_as_float(hv[i].w << 16) + bfr(s1[2]), __uint_as_float(hv[i].w & 0xffff0000u) + bfr(s1[3]));
    *(uint4*)(C + (size_t)(m0 + row) * D_ + n0 + ch * 8) = o;
  }
  STAMP(4, 4);
}

extern "C" void kernel_launch(void* const* d_in, const int* in_sizes, int n_in, void* d_out, int out_size, void* d_ws, size_t ws_size,
                              hipStream_t stream) {
  const u16* x = (const u16*)d_in[0];
  const u16* wr = (const u16*)d_in[1];
  const u16* wi = (const u16*)d_in[2];
  const u16* w1 = (const u16*)d_in[4];
  const u16* b1 = (const u16*)d_in[5];
  const u16* w2 = (const u16*)d_in[6];
  char* ws = (char*)d_ws;
  float2* T = (float2*)ws;
  u32* Wt = (u32*)(ws + (8u << 20));
  u16* h = (u16*)(ws + (12u << 20));
  u16* ln2 = (u16*)(ws + (16u << 20));
  u16* act = (u16*)(ws + (20u << 20));
  float4* Cs = (float4*)(ws + (36u << 20));
  (void)hipFuncSetAttribute((const void*)k4_gemm1, hipFuncAttributeMaxDynamicSharedMemorySize, G1_STAGE * G1_NST);
  (void)hipFuncSetAttribute((const void*)k5_gemm2, hipFuncAttributeMaxDynamicSharedMemorySize, G2_STAGE * G2_NST);
  k1_ln_rfft<<<256, 512, 0, stream>>>(x, wr, wi, T, Wt, Cs);
  k2_col<<<256, 512, 0, stream>>>(T, Wt, Cs);
  k3_irfft_ln<<<256, 512, 0, stream>>>(T, x, h, ln2);
  k4_gemm1<<<256, 512, G1_STAGE * G1_NST, stream>>>(ln2, w1, b1, act);
  k5_gemm2<<<256, 512, G2_STAGE * G2_NST, stream>>>(act, w2, h, (u16*)d_out);
}
```

```cpp
#include <hip/hip_runtime.h>
#include <hip/hip_bf16.h>

typedef unsigned short u16;
typedef unsigned int u32;
typedef short s16x4 __attribute__((ext_vector_type(4)));
typedef short s16x8 __attribute__((ext_vector_type(8)));
typedef __bf16 bf16x8 __attribute__((ext_vector_type(8)));
typedef float f32x4 __attribute__((ext_vector_type(4)));
typedef u32 u32x2 __attribute__((ext_vector_type(2)));
#define LDS_AS __attribute__((address_space(3)))

#define STAMP(k, i)
constexpr int S_ = 2048, D_ = 1024, F_ = 3584, DF_ = 513;
#define LDACT 3648
constexpr int LA_ = LDACT;

__device__ __forceinline__ float bf2f(u16 v) { return __uint_as_float(((u32)v) << 16); }
__device__ __forceinline__ u16 f2bf(float f) { return __builtin_bit_cast(u16, (__bf16)f); }
__device__ __forceinline__ float bfr(float f) { return bf2f(f2bf(f)); }
__device__ __forceinline__ u32 pk2(float lo, float hi) { return (u32)f2bf(lo) | ((u32)f2bf(hi) << 16); }

__device__ __forceinline__ float2 cadd(float2 a, float2 b) { return make_float2(a.x + b.x, a.y + b.y); }
__device__ __forceinline__ float2 csub(float2 a, float2 b) { return make_float2(a.x - b.x, a.y - b.y); }
__device__ __forceinline__ float2 cmul(float2 a, float2 b) { return make_float2(a.x * b.x - a.y * b.y, a.x * b.y + a.y * b.x); }
__device__ __forceinline__ float2 cmulc(float2 a, float2 b) { return make_float2(a.x * b.x + a.y * b.y, a.y * b.x - a.x * b.y); }
template <int SGN> __device__ __forceinline__ float2 twm(float2 a, float2 w) { return SGN < 0 ? cmul(a, w) : cmulc(a, w); }
template <int SGN> __device__ __forceinline__ float2 muli(float2 a) { return SGN < 0 ? make_float2(a.y, -a.x) : make_float2(-a.y, a.x); }

template <int SGN> __device__ __forceinline__ void dft4(float2& b0, float2& b1, float2& b2, float2& b3) {
  float2 s02 = cadd(b0, b2), d02 = csub(b0, b2), s13 = cadd(b1, b3), d13 = muli<SGN>(csub(b1, b3));
  b0 = cadd(s02, s13); b2 = csub(s02, s13); b1 = cadd(d02, d13); b3 = csub(d02, d13);
}
template <int SGN> __device__ __forceinline__ void dft8(float2 (&a)[8]) {
  const float R = 0.70710678118654752440f;
  float2 e0 = cadd(a[0], a[4]), e1 = cadd(a[1], a[5]), e2 = cadd(a[2], a[6]), e3 = cadd(a[3], a[7]);
  float2 o0 = csub(a[0], a[4]), o1 = csub(a[1], a[5]), o2 = csub(a[2], a[6]), o3 = csub(a[3], a[7]);
  if (SGN < 0) {
    o1 = make_float2((o1.x + o1.y) * R, (o1.y - o1.x) * R);
    o3 = make_float2((o3.y - o3.x) * R, -(o3.x + o3.y) * R);
  } else {
    o1 = make_float2((o1.x - o1.y) * R, (o1.x + o1.y) * R);
    o3 = make_float2(-(o3.x + o3.y) * R, (o3.x - o3.y) * R);
  }
  o2 = muli<SGN>(o2);
  dft4<SGN>(e0, e1, e2, e3);
  dft4<SGN>(o0, o1, o2, o3);
  a[0] = e0; a[2] = e1; a[4] = e2; a[6] = e3;
  a[1] = o0; a[3] = o1; a[5] = o2; a[7] = o3;
}

__device__ __forceinline__ float wave_sum(float v) {
  v += __builtin_bit_cast(float, __builtin_amdgcn_update_dpp(0, __builtin_bit_cast(int, v), 0x111, 0xf, 0xf, true));
  v += __builtin_bit_cast(float, __builtin_amdgcn_update_dpp(0, __builtin_bit_cast(int, v), 0x112, 0xf, 0xf, true));
  v += __builtin_bit_cast(float, __builtin_amdgcn_update_dpp(0, __builtin_bit_cast(int, v), 0x114, 0xf, 0xf, true));
  v += __builtin_bit_cast(float, __builtin_amdgcn_update_dpp(0, __builtin_bit_cast(int, v), 0x118, 0xf, 0xf, true));
  v += __builtin_bit_cast(float, __builtin_amdgcn_update_dpp(0, __builtin_bit_cast(int, v), 0x142, 0xa, 0xf, true));
  v += __builtin_bit_cast(float, __builtin_amdgcn_update_dpp(0, __builtin_bit_cast(int, v), 0x143, 0xc, 0xf, true));
  return __builtin_bit_cast(float, __builtin_amdgcn_readlane(__builtin_bit_cast(int, v), 63));
}
__device__ __forceinline__ float2 cispi_neg(float x) { float s, c; sincospif(x, &s, &c); return make_float2(c, -s); }
__device__ __forceinline__ void pow7(float2 w, float2 (&p)[8]) {
  p[0] = make_float2(1.f, 0.f);
  p[1] = w; p[2] = cmul(w, w); p[3] = cmul(p[2], w); p[4] = cmul(p[2], p[2]);
  p[5] = cmul(p[4], w); p[6] = cmul(p[3], p[3]); p[7] = cmul(p[4], p[3]);
}
#define LGKM0() do { __builtin_amdgcn_s_waitcnt(0xC07F); __builtin_amdgcn_wave_barrier(); } while (0)

template <int SGN> __device__ __forceinline__ void fft512_wave(float2 (&a)[8], float2* bw, int lane) {
  const int l0 = lane & 7, mm = lane >> 3;
  float2 p1[8], p2[8];
  pow7(cispi_neg((float)lane * (1.0f / 256.0f)), p1);
  pow7(cispi_neg((float)l0 * (1.0f / 32.0f)), p2);
  dft8<SGN>(a);
#pragma unroll
  for (int m = 1; m < 8; ++m) a[m] = twm<SGN>(a[m], p1[m]);
#pragma unroll
  for (int m = 0; m < 8; ++m) bw[m * 72 + lane] = a[m];
  LGKM0();
#pragma unroll
  for (int j = 0; j < 8; ++j) a[j] = bw[mm * 72 + l0 + 8 * j];
  LGKM0();
  dft8<SGN>(a);
#pragma unroll
  for (int m = 1; m < 8; ++m) a[m] = twm<SGN>(a[m], p2[m]);
#pragma unroll
  for (int m = 0; m < 8; ++m) bw[l0 * 66 + mm + 8 * m] = a[m];
  LGKM0();
#pragma unroll
  for (int j = 0; j < 8; ++j) a[j] = bw[j * 66 + lane];
  LGKM0();
  dft8<SGN>(a);
}

constexpr int RS_ = 584;

__device__ __forceinline__ int k2_freq(int i, int tau) {
  const int h = tau & 3, m2 = (tau >> 2) & 7, m1 = tau >> 5, e = i >> 2, m4 = i & 3;
  return m1 + 8 * m2 + 64 * (2 * h + e) + 512 * m4;
}
__device__ __forceinline__ float2 rot8(float2 w, int it) {
  const float R = 0.70710678118654752440f;
  return it == 0 ? w : it == 1 ? make_float2((w.x + w.y) * R, (w.y - w.x) * R) : it == 2 ? make_float2(w.y, -w.x) : make_float2((w.y - w.x) * R, -(w.x + w.y) * R);
}

__global__ __launch_bounds__(512) void k1_ln_rfft(const u16* __restrict__ x, const u16* __restrict__ wr, const u16* __restrict__ wi,
                                                  float2* __restrict__ T, u32* __restrict__ Wt, float4* __restrict__ Cs, const u32* __restrict__ pf1) {
  __shared__ __attribute__((aligned(16))) float2 buf[8 * RS_];
  const int tid = threadIdx.x, lane = tid & 63, w = tid >> 6;
  const int r = tid & 7;
  if (blockIdx.x >= 256) {
    const int blk = blockIdx.x - 256;
    u32 (*wsm)[516] = (u32 (*)[516])buf;
    const int f = k2_freq(blk >> 5, ((blk & 31) << 3) + w);
    const u16* pr = wr + (size_t)f * DF_;
    const u16* pi = wi + (size_t)f * DF_;
    u16 wra[8], wia[8];
#pragma unroll
    for (int j = 0; j < 8; ++j) { wra[j] = pr[lane + 64 * j]; wia[j] = pi[lane + 64 * j]; }
    const u32 pfa = pf1[(blk * 448 + (tid < 448 ? tid : 447)) * 16];
    if (lane == 0) {
      const int fm = (2048 - f) & 2047;
      const float w0r = bf2f(pr[0]), w0i = bf2f(pi[0]), w0mr = bf2f(wr[(size_t)fm * DF_]), w0mi = bf2f(wi[(size_t)fm * DF_]);
      const float wnr = bf2f(pr[512]), wni = bf2f(pi[512]), wnmr = bf2f(wr[(size_t)fm * DF_ + 512]), wnmi = bf2f(wi[(size_t)fm * DF_ + 512]);
      const float2 A = make_float2(0.5f * (w0r + w0mr), 0.5f * (w0i - w0mi));
      const float2 B = make_float2(0.5f * (wnr + wnmr), 0.5f * (wni - wnmi));
      Cs[(blk >> 5) * 256 + ((blk & 31) << 3) + w] = make_float4(0.5f * (A.x + B.x), 0.5f * (A.y + B.y), 0.5f * (A.x - B.x), 0.5f * (A.y - B.y));
    }
#pragma unroll
    for (int j = 0; j < 8; ++j) wsm[w][lane + 64 * j] = (u32)wra[j] | ((u32)wia[j] << 16);
    __syncthreads();
    const int i = blk >> 5, tau0 = (blk & 31) << 3;
#pragma unroll
    for (int it = 0; it < 4; ++it) {
      const int kp = (tid >> 3) + 64 * it;
      u32x2 v; v.x = wsm[r][2 * kp]; v.y = wsm[r][2 * kp + 1];
      *(u32x2*)(Wt + ((size_t)kp * 8 + i) * 512 + 2 * (tau0 + r)) = v;
    }
    asm volatile("" ::"v"(pfa));
    return;
  }
  const int blk = blockIdx.x;
  const int row = blk * 8 + w;
  STAMP(0, 0);
  const u32* xr = (const u32*)(x + (size_t)row * D_);
  u32 raw[8];
#pragma unroll
  for (int j = 0; j < 8; ++j) raw[j] = xr[lane + 64 * j];
  const float2 wpost = cispi_neg((float)(tid >> 3) * (1.0f / 256.0f));
  float2 a[8];
  float sum = 0.f;
#pragma unroll
  for (int j = 0; j < 8; ++j) { a[j] = make_float2(__uint_as_float(raw[j] << 16), __uint_as_float(raw[j] & 0xffff0000u)); sum += a[j].x + a[j].y; }
  const float mean = wave_sum(sum) * (1.0f / 1024.0f);
  float sq = 0.f;
#pragma unroll
  for (int j = 0; j < 8; ++j) { const float dx = a[j].x - mean, dy = a[j].y - mean; sq += dx * dx + dy * dy; }
  const float var = wave_sum(sq) * (1.0f / 1024.0f);
  const float mb = bfr(mean), rden = 1.0f / bfr(sqrtf(bfr(bfr(var) + 1e-5f)));
#pragma unroll
  for (int j = 0; j < 8; ++j) { a[j].x = bfr(bfr(a[j].x - mb) * rden); a[j].y = bfr(bfr(a[j].y - mb) * rden); }
  STAMP(0, 1);
  float2* bw = buf + w * RS_;
  fft512_wave<-1>(a, bw, lane);
#pragma unroll
  for (int m = 0; m < 8; ++m) bw[lane + 64 * m] = a[m];
  __syncthreads();
  STAMP(0, 2);
  const float2* zr = buf + r * RS_;
  const float2 w1 = make_float2(0.99998117528260111f, -0.0061358846491544753f);
#pragma unroll
  for (int it = 0; it < 4; ++it) {
    const int kp = (tid >> 3) + 64 * it;
    const float2 we = rot8(wpost, it);
    float4 o;
#pragma unroll
    for (int e = 0; e < 2; ++e) {
      const int k = 2 * kp + e;
      const float2 zk = zr[k], zm = zr[(512 - k) & 511];
      const float2 sum2 = make_float2(zk.x + zm.x, zk.y - zm.y);
      const float2 dif2 = make_float2(zk.x - zm.x, zk.y + zm.y);
      const float2 t = cmul(dif2, e ? cmul(we, w1) : we);
      float2 X = make_float2(sum2.x + t.y, sum2.y - t.x);
      if (k == 0) X = make_float2(2.f * (zk.x + zk.y), 2.f * (zk.x - zk.y));
      if (e == 0) { o.x = X.x; o.y = X.y; } else { o.z = X.x; o.w = X.y; }
    }
    *(float4*)(T + ((size_t)kp * S_ + blk * 8 + r) * 2) = o;
  }
  STAMP(0, 3);
  STAMP(0, 4);
}

__device__ __forceinline__ int ex3(int g, int m, int v, int c) { return g * 74 + (m * 4 + v) * 2 + c + (m >> 2) * 8; }
__global__ __launch_bounds__(512) void k2_col(float2* __restrict__ T, const u32* __restrict__ Wt, const float4* __restrict__ Cs,
                                              const u32* __restrict__ pf2) {
  __shared__ __attribute__((aligned(16))) float2 exA[4736];
  __shared__ __attribute__((aligned(16))) float2 exB[4736];
  const int t = threadIdx.x, kp = blockIdx.x, c = t & 1, u = t >> 1;
  float2* col = T + (size_t)kp * S_ * 2;
  STAMP(1, 0);
  float2 a[8];
#pragma unroll
  for (int j = 0; j < 8; ++j) a[j] = col[(u + 256 * j) * 2 + c];
  u32 wv[8];
#pragma unroll
  for (int i = 0; i < 8; ++i) wv[i] = Wt[((size_t)kp * 8 + i) * 512 + t];
  float4 cs[8];
  if (kp == 0) {
#pragma unroll
    for (int i = 0; i < 8; ++i) cs[i] = Cs[i * 256 + u];
  }
  const int u0 = u & 31, m1 = u >> 5;
  const int v0 = u & 3, g = u >> 2;
  float2 p1[8], p2[8], p3[8];
  pow7(cispi_neg((float)u * (1.0f / 1024.0f)), p1);
  pow7(cispi_neg((float)u0 * (1.0f / 128.0f)), p2);
  pow7(cispi_neg((float)v0 * (1.0f / 16.0f)), p3);
  STAMP(1, 1);
  dft8<-1>(a);
#pragma unroll
  for (int m = 1; m < 8; ++m) a[m] = cmul(a[m], p1[m]);
#pragma unroll
  for (int m = 0; m < 8; ++m) exA[(m * 256 + u) * 2 + c] = a[m];
  asm volatile("s_waitcnt vmcnt(0)" ::: "memory");
  __syncthreads();
  const int pfi = (kp * 448 + (t < 448 ? t : 447)) * 16;
  const u32 pfb = pf2[pfi];
#pragma unroll
  for (int j = 0; j < 8; ++j) a[j] = exA[(m1 * 256 + u0 + 32 * j) * 2 + c];
  dft8<-1>(a);
#pragma unroll
  for (int m = 1; m < 8; ++m) a[m] = cmul(a[m], p2[m]);
#pragma unroll
  for (int m = 0; m < 8; ++m) exB[((m1 * 8 + m) * 36 + u0) * 2 + c] = a[m];
  __syncthreads();
#pragma unroll
  for (int j = 0; j < 8; ++j) a[j] = exB[(g * 36 + v0 + 4 * j) * 2 + c];
  dft8<-1>(a);
#pragma unroll
  for (int m = 1; m < 8; ++m) a[m] = cmul(a[m], p3[m]);
#pragma unroll
  for (int m = 0; m < 8; ++m) exA[ex3(g, m, v0, c)] = a[m];
  __syncthreads();
#pragma unroll
  for (int e = 0; e < 2; ++e)
#pragma unroll
    for (int v = 0; v < 4; ++v) a[e * 4 + v] = exA[ex3(g, 2 * v0 + e, v, c)];
  dft4<-1>(a[0], a[1], a[2], a[3]);
  dft4<-1>(a[4], a[5], a[6], a[7]);
  STAMP(1, 2);
  if (kp == 0) {
    if (c == 0) {
#pragma unroll
      for (int i = 0; i < 8; ++i) exB[k2_freq(i, u)] = a[i];
    }
    __syncthreads();
    if (c == 0) {
#pragma unroll
      for (int i = 0; i < 8; ++i) {
        const int f = k2_freq(i, u), fm = (2048 - f) & 2047;
        const float2 pm = exB[fm];
        const float2 C1 = make_float2(cs[i].x, cs[i].y), C2 = make_float2(cs[i].z, cs[i].w);
        a[i] = cadd(cmul(a[i], C1), cmul(make_float2(pm.x, -pm.y), C2));
      }
    } else {
#pragma unroll
      for (int i = 0; i < 8; ++i) a[i] = cmul(a[i], make_float2(__uint_as_float(wv[i] << 16), __uint_as_float(wv[i] & 0xffff0000u)));
    }
    __syncthreads();
  } else {
#pragma unroll
    for (int i = 0; i < 8; ++i) a[i] = cmul(a[i], make_float2(__uint_as_float(wv[i] << 16), __uint_as_float(wv[i] & 0xffff0000u)));
  }
  dft4<1>(a[0], a[1], a[2], a[3]);
  dft4<1>(a[4], a[5], a[6], a[7]);
#pragma unroll
  for (int e = 0; e < 2; ++e)
#pragma unroll
    for (int v = 0; v < 4; ++v) exB[ex3(g, 2 * v0 + e, v, c)] = a[e * 4 + v];
  __syncthreads();
#pragma unroll
  for (int m = 0; m < 8; ++m) a[m] = exB[ex3(g, m, v0, c)];
#pragma unroll
  for (int m = 1; m < 8; ++m) a[m] = cmulc(a[m], p3[m]);
  dft8<1>(a);
#pragma unroll
  for (int j = 0; j < 8; ++j) exA[(g * 36 + v0 + 4 * j) * 2 + c] = a[j];
  __syncthreads();
#pragma unroll
  for (int m = 0; m < 8; ++m) a[m] = exA[((m1 * 8 + m) * 36 + u0) * 2 + c];
#pragma unroll
  for (int m = 1; m < 8; ++m) a[m] = cmulc(a[m], p2[m]);
  dft8<1>(a);
#pragma unroll
  for (int j = 0; j < 8; ++j) exB[(m1 * 256 + u0 + 32 * j) * 2 + c] = a[j];
  __syncthreads();
#pragma unroll
  for (int m = 0; m < 8; ++m) a[m] = exB[(m * 256 + u) * 2 + c];
#pragma unroll
  for (int m = 1; m < 8; ++m) a[m] = cmulc(a[m], p1[m]);
  dft8<1>(a);
#pragma unroll
  for (int j = 0; j < 8; ++j) col[(u + 256 * j) * 2 + c] = a[j];
  asm volatile("" ::"v"(pfb));
  STAMP(1, 3);
  STAMP(1, 4);
}

__global__ __launch_bounds__(512) void k3_irfft_ln(const float2* __restrict__ T, const u16* __restrict__ x, u16* __restrict__ hout, u16* __restrict__ ln2) {
  __shared__ __attribute__((aligned(16))) float2 buf[8 * RS_];
  const int tid = threadIdx.x, lane = tid & 63, w = tid >> 6, blk = blockIdx.x;
  const int row = blk * 8 + w;
  STAMP(2, 0);
  {
    const int r = tid & 7;
    float4 v[4];
#pragma unroll
    for (int it = 0; it < 4; ++it) v[it] = *(const float4*)(T + ((size_t)((tid >> 3) + 64 * it) * S_ + blk * 8 + r) * 2);
#pragma unroll
    for (int it = 0; it < 4; ++it) *(float4*)(buf + r * RS_ + 2 * ((tid >> 3) + 64 * it)) = v[it];
  }
  const u32* xr = (const u32*)(x + (size_t)row * D_);
  u32 raw[8];
#pragma unroll
  for (int j = 0; j < 8; ++j) raw[j] = xr[lane + 64 * j];
  const float2 wl = cispi_neg((float)lane * (1.0f / 512.0f));
  __syncthreads();
  STAMP(2, 1);
  float2* bw = buf + w * RS_;
  float2 a[8];
  const float C8 = 0.92387953251128674f, S8 = 0.38268343236508977f;
  const float2 wl2 = cmul(wl, make_float2(C8, -S8));
#pragma unroll
  for (int j = 0; j < 8; ++j) {
    const int k = lane + 64 * j;
    const float2 yk = bw[k], ym = bw[(512 - k) & 511];
    const float2 sum2 = make_float2(yk.x + ym.x, yk.y - ym.y);
    const float2 dif2 = make_float2(yk.x - ym.x, yk.y + ym.y);
    const float2 t = cmulc(dif2, rot8((j & 1) ? wl2 : wl, j >> 1));
    a[j] = make_float2(sum2.x - t.y, sum2.y + t.x);
    if (k == 0) a[j] = make_float2(yk.x + yk.y, yk.x - yk.y);
  }
  LGKM0();
  fft512_wave<1>(a, bw, lane);
  STAMP(2, 2);
  const float scale = 1.0f / 4194304.0f;
  float sum = 0.f;
#pragma unroll
  for (int j = 0; j < 8; ++j) {
    a[j].x = bfr(__uint_as_float(raw[j] << 16) + bfr(a[j].x * scale));
    a[j].y = bfr(__uint_as_float(raw[j] & 0xffff0000u) + bfr(a[j].y * scale));
    sum += a[j].x + a[j].y;
  }
  u32* ho = (u32*)(hout + (size_t)row * D_);
  u32* lo = (u32*)(ln2 + (size_t)row * D_);
#pragma unroll
  for (int j = 0; j < 8; ++j) ho[lane + 64 * j] = pk2(a[j].x, a[j].y);
  const float mean = wave_sum(sum) * (1.0f / 1024.0f);
  float sq = 0.f;
#pragma unroll
  for (int j = 0; j < 8; ++j) { const float dx = a[j].x - mean, dy = a[j].y - mean; sq += dx * dx + dy * dy; }
  const float var = wave_sum(sq) * (1.0f / 1024.0f);
  const float mb = bfr(mean), rden = 1.0f / bfr(sqrtf(bfr(bfr(var) + 1e-5f)));
#pragma unroll
  for (int j = 0; j < 8; ++j) lo[lane + 64 * j] = pk2(bfr(a[j].x - mb) * rden, bfr(a[j].y - mb) * rden);
  STAMP(2, 3);
  STAMP(2, 4);
}

#define WAIT_VM(n) asm volatile("s_waitcnt vmcnt(%0)" ::"n"(n) : "memory")
#define BARRIER() do { asm volatile("s_waitcnt lgkmcnt(0)" ::: "memory"); __builtin_amdgcn_s_barrier(); asm volatile("" ::: "memory"); } while (0)

typedef int i32x4 __attribute__((ext_vector_type(4)));
__device__ __forceinline__ i32x4 mk_rsrc(const void* p, u32 bytes) {
  const unsigned long long a = (unsigned long long)p;
  i32x4 r; r.x = (int)(u32)a; r.y = (int)(u32)(a >> 32); r.z = (int)bytes; r.w = 0x00020000; return r;
}
__device__ __forceinline__ void dma16(i32x4 rs, u32 lds_addr, int voff, int soff) {
  asm volatile("s_mov_b32 m0, %0\n\ts_nop 0\n\tbuffer_load_dwordx4 %1, %2, %3 offen lds" ::"s"(lds_addr), "v"(voff), "s"(rs), "s"(soff) : "memory");
}
__device__ __forceinline__ bf16x8 lds_a(const char* p) { return *(const bf16x8*)p; }
__device__ __forceinline__ bf16x8 lds_tr(const char* p0, const char* p1) {
  s16x4 lo = __builtin_amdgcn_ds_read_tr16_b64_v4i16((LDS_AS s16x4*)p0);
  s16x4 hi = __builtin_amdgcn_ds_read_tr16_b64_v4i16((LDS_AS s16x4*)p1);
  s16x8 v = __builtin_shufflevector(lo, hi, 0, 1, 2, 3, 4, 5, 6, 7);
  return __builtin_bit_cast(bf16x8, v);
}
typedef float f32x2 __attribute__((ext_vector_type(2)));
__device__ __forceinline__ f32x2 mish2(f32x2 h) {
  f32x2 t; t.x = fminf(h.x, 30.f); t.y = fminf(h.y, 30.f);
  t = t * 1.4426950408889634f;
  f32x2 u; u.x = __builtin_amdgcn_exp2f(t.x); u.y = __builtin_amdgcn_exp2f(t.y);
  const f32x2 n = u * (u + 2.f), d = n + 2.f;
  f32x2 r; r.x = __builtin_amdgcn_rcpf(d.x); r.y = __builtin_amdgcn_rcpf(d.y);
  return (h * n) * r;
}

constexpr int G1_STAGE = 16384 + 28672, G1_NST = 3;
__global__ __launch_bounds__(512, 2) void k4_gemm1(const u16* __restrict__ A, const u16* __restrict__ B, const u16* __restrict__ bias, u16* __restrict__ C) {
  extern __shared__ __attribute__((aligned(16))) char sm[];
  const int tid = threadIdx.x, lane = tid & 63, wv = __builtin_amdgcn_readfirstlane(tid >> 6);
  const u32 smbase = (u32)(size_t)(LDS_AS char*)sm;
  const int xcd = blockIdx.x & 7, idx = blockIdx.x >> 3;
  const int tm = (xcd & 1) * 8 + (idx & 7), tn = (xcd >> 1) * 4 + (idx >> 3);
  const int m0 = tm * 128, n0 = tn * 224;
  STAMP(3, 0);
  constexpr int NT = D_ / 64;
  if (wv >= 4) {
    const int w = wv - 4;
    const i32x4 rsA = mk_rsrc(A, S_ * D_ * 2);
    const i32x4 rsB = mk_rsrc(B, D_ * F_ * 2);
    int voA[4], voB[7];
#pragma unroll
    for (int i = 0; i < 4; ++i) { const int row = (w * 4 + i) * 8 + (lane >> 3), cch = (lane & 7) ^ (row & 7); voA[i] = (m0 + row) * (D_ * 2) + cch * 16; }
#pragma unroll
    for (int i = 0; i < 7; ++i) {
      const int L = (w * 7 + i) * 64 + lane, k = L / 28, cc = L % 28, nb = (cc >> 1) ^ ((k >> 3) & 1);
      voB[i] = k * (F_ * 2) + (n0 + nb * 16 + (cc & 1) * 8) * 2;
    }
#define G1_STAGE_LOAD(st, kt) do { const u32 sb_ = smbase + (st) * G1_STAGE; \
    _Pragma("unroll") for (int i = 0; i < 4; ++i) dma16(rsA, sb_ + (w * 4 + i) * 1024, voA[i], (kt) * 128); \
    _Pragma("unroll") for (int i = 0; i < 7; ++i) dma16(rsB, sb_ + 16384 + (w * 7 + i) * 1024, voB[i], (kt) * (64 * F_ * 2)); } while (0)
    G1_STAGE_LOAD(0, 0);
    G1_STAGE_LOAD(1, 1);
    G1_STAGE_LOAD(2, 2);
    WAIT_VM(22);
    BARRIER();
    int st = 0;
    for (int kt = 0; kt < NT; ++kt) {
      if (kt + 2 < NT) WAIT_VM(11); else WAIT_VM(0);
      BARRIER();
      if (kt + 3 < NT) G1_STAGE_LOAD(st, kt + 3);
      st = st == 2 ? 0 : st + 1;
    }
  } else {
    const int wr = wv >> 1, wc = wv & 1, fr = lane & 15, fq = lane >> 4;
    f32x4 acc[4][7];
#pragma unroll
    for (int i = 0; i < 4; ++i)
#pragma unroll
      for (int j = 0; j < 7; ++j) acc[i][j] = (f32x4){0.f, 0.f, 0.f, 0.f};
    const int aoff = (wr * 64 + fr) * 128;
    const int ach0 = ((0 + fq) ^ (fr & 7)) * 16, ach1 = ((4 + fq) ^ (fr & 7)) * 16;
    const int q = (lane & 15) >> 2, p = lane & 3;
    const int boff = 16384 + (8 * fq + q) * 448 + p * 8;
    const int gb = fq & 1;
    bf16x8 ax[4], bx[7], ay[4], by[7];
#define G1_READ(AF, BF, sb, ks) do { \
    _Pragma("unroll") for (int mi = 0; mi < 4; ++mi) AF[mi] = lds_a((sb) + aoff + mi * 2048 + ((ks) ? ach1 : ach0)); \
    _Pragma("unroll") for (int ni = 0; ni < 7; ++ni) { const char* bp = (sb) + boff + (ks) * (32 * 448) + (((wc * 7 + ni) ^ gb) * 32); BF[ni] = lds_tr(bp, bp + 4 * 448); } } while (0)
#define G1_MMA(AF, BF) do { \
    _Pragma("unroll") for (int ni = 0; ni < 7; ++ni) _Pragma("unroll") for (int mi = 0; mi < 4; ++mi) \
      acc[mi][ni] = __builtin_amdgcn_mfma_f32_16x16x32_bf16(BF[ni], AF[mi], acc[mi][ni], 0, 0, 0); } while (0)
#define G1_SCHED() do { _Pragma("unroll") for (int i = 0; i < 18; ++i) { __builtin_amdgcn_sched_group_barrier(0x008, 1, 0); __builtin_amdgcn_sched_group_barrier(0x100, 1, 0); } \
    __builtin_amdgcn_sched_group_barrier(0x008, 10, 0); __builtin_amdgcn_sched_barrier(0); } while (0)
    BARRIER();
    G1_READ(ax, bx, sm, 0);
    STAMP(3, 1);
    int st = 0;
    for (int kt = 0; kt < NT; ++kt) {
      const char* sb = sm + st * G1_STAGE;
      const int sn = st == 2 ? 0 : st + 1;
      __builtin_amdgcn_sched_barrier(0);
      G1_READ(ay, by, sb, 1);
      G1_MMA(ax, bx);
      G1_SCHED();
      BARRIER();
      __builtin_amdgcn_sched_barrier(0);
      G1_READ(ax, bx, sm + sn * G1_STAGE, 0);
      G1_MMA(ay, by);
      G1_SCHED();
      st = sn;
    }
    STAMP(3, 2);
#pragma unroll
    for (int ni = 0; ni < 7; ++ni)
#pragma unroll
      for (int mi = 0; mi < 4; ++mi) *(f32x4*)(sm + (wr * 64 + mi * 16 + fr) * 912 + (wc * 112 + ni * 16 + fq * 4) * 4) = acc[mi][ni];
  }
  BARRIER();
  STAMP(3, 3);
#pragma unroll
  for (int i = 0; i < 7; ++i) {
    const int id = tid + 512 * i, row = id / 28, ch = id % 28;
    const f32x4 v0 = *(const f32x4*)(sm + row * 912 + ch * 32), v1 = *(const f32x4*)(sm + row * 912 + ch * 32 + 16);
    const uint4 bb = *(const uint4*)(bias + n0 + ch * 8);
    const f32x2 r0 = mish2((f32x2){v0[0] + __uint_as_float(bb.x << 16), v0[1] + __uint_as_float(bb.x & 0xffff0000u)});
    const f32x2 r1 = mish2((f32x2){v0[2] + __uint_as_float(bb.y << 16), v0[3] + __uint_as_float(bb.y & 0xffff0000u)});
    const f32x2 r2 = mish2((f32x2){v1[0] + __uint_as_float(bb.z << 16), v1[1] + __uint_as_float(bb.z & 0xffff0000u)});
    const f32x2 r3 = mish2((f32x2){v1[2] + __uint_as_float(bb.w << 16), v1[3] + __uint_as_float(bb.w & 0xffff0000u)});
    uint4 o; o.x = pk2(r0.x, r0.y); o.y = pk2(r1.x, r1.y); o.z = pk2(r2.x, r2.y); o.w = pk2(r3.x, r3.y);
    *(uint4*)((char*)C + (size_t)(m0 + row) * (LA_ * 2) + n0 * 2 + ch * 16) = o;
  }
  STAMP(3, 4);
}

constexpr int G2_STAGE = 32768 + 16384, G2_NST = 3;
__global__ __launch_bounds__(512, 2) void k5_gemm2(const u16* __restrict__ A, const u16* __restrict__ B, const u16* __restrict__ H, u16* __restrict__ C) {
  extern __shared__ __attribute__((aligned(16))) char sm[];
  const int tid = threadIdx.x, lane = tid & 63, wv = __builtin_amdgcn_readfirstlane(tid >> 6);
  const u32 smbase = (u32)(size_t)(LDS_AS char*)sm;
  const int xcd = blockIdx.x & 7, idx = blockIdx.x >> 3;
  const int tm = (xcd >> 1) * 4 + (idx & 3), tn = (xcd & 1) * 8 + (idx >> 2);
  const int m0 = tm * 128, n0 = tn * 64;
  STAMP(4, 0);
  constexpr int NT = F_ / 128;
  if (wv >= 4) {
    const int w = wv - 4;
    const i32x4 rsA = mk_rsrc(A, S_ * LA_ * 2);
    const i32x4 rsB = mk_rsrc(B, F_ * D_ * 2);
    int voA[8], voB[4];
#pragma unroll
    for (int i = 0; i < 8; ++i) { const int pc = w * 8 + i, sub = pc >> 4, row = (pc & 15) * 8 + (lane >> 3), cch = (lane & 7) ^ (row & 7); voA[i] = (m0 + row) * (LA_ * 2) + sub * 128 + cch * 16; }
#pragma unroll
    for (int i = 0; i < 4; ++i) {
      const int L = (w * 4 + i) * 64 + lane, k = L >> 3, cc = L & 7, f = ((k >> 1) & 1) | (((k >> 3) & 1) << 1), nb = (cc >> 1) ^ f;
      voB[i] = k * (D_ * 2) + (n0 + nb * 16 + (cc & 1) * 8) * 2;
    }
#define G2_STAGE_LOAD(st, kt) do { const u32 sb_ = smbase + (st) * G2_STAGE; \
    _Pragma("unroll") for (int i = 0; i < 8; ++i) dma16(rsA, sb_ + (w * 8 + i) * 1024, voA[i], (kt) * 256); \
    _Pragma("unroll") for (int i = 0; i < 4; ++i) dma16(rsB, sb_ + 32768 + (w * 4 + i) * 1024, voB[i], (kt) * (128 * D_ * 2)); } while (0)
    G2_STAGE_LOAD(0, 0);
    G2_STAGE_LOAD(1, 1);
    G2_STAGE_LOAD(2, 2);
    WAIT_VM(24);
    BARRIER();
    int st = 0;
    for (int kt = 0; kt < NT; ++kt) {
      if (kt + 2 < NT) WAIT_VM(12); else WAIT_VM(0);
      BARRIER();
      if (kt + 3 < NT) G2_STAGE_LOAD(st, kt + 3);
      st = st == 2 ? 0 : st + 1;
    }
    BARRIER();
  } else {
    const int w = wv, fr = lane & 15, fq = lane >> 4;
    f32x4 acc[8][4];
#pragma unroll
    for (int i = 0; i < 8; ++i)
#pragma unroll
      for (int j = 0; j < 4; ++j) acc[i][j] = (f32x4){0.f, 0.f, 0.f, 0.f};
    const int aoff = (w >> 1) * 16384 + fr * 128 + ((((w & 1) * 4 + fq) ^ (fr & 7)) * 16);
    const int q = (lane & 15) >> 2, p = lane & 3;
    const int bfx = ((q >> 1) & 1) | ((fq & 1) << 1);
    const int boff = 32768 + (32 * w + 8 * fq + q) * 128 + p * 8;
    bf16x8 ax[8], bx[4], ay[8], by[4];
#define G2_READ(AF, BF, sb) do { \
    _Pragma("unroll") for (int ni = 0; ni < 4; ++ni) { const char* bp = (sb) + boff + ((ni ^ bfx) * 32); BF[ni] = lds_tr(bp, bp + 512); } \
    _Pragma("unroll") for (int mi = 0; mi < 8; ++mi) AF[mi] = lds_a((sb) + aoff + mi * 2048); } while (0)
#define G2_MMA(AF, BF) do { \
    _Pragma("unroll") for (int mi = 0; mi < 8; ++mi) _Pragma("unroll") for (int ni = 0; ni < 4; ++ni) \
      acc[mi][ni] = __builtin_amdgcn_mfma_f32_16x16x32_bf16(BF[ni], AF[mi], acc[mi][ni], 0, 0, 0); } while (0)
#define G2_SCHED() do { _Pragma("unroll") for (int i = 0; i < 16; ++i) { __builtin_amdgcn_sched_group_barrier(0x008, 1, 0); __builtin_amdgcn_sched_group_barrier(0x100, 1, 0); } \
    __builtin_amdgcn_sched_group_barrier(0x008, 16, 0); __builtin_amdgcn_sched_barrier(0); } while (0)
#define G2_ITER(AC, BC, AN, BN) do { \
    const int sn = st == 2 ? 0 : st + 1; \
    BARRIER(); \
    __builtin_amdgcn_sched_barrier(0); \
    G2_READ(AN, BN, sm + sn * G2_STAGE); \
    G2_MMA(AC, BC); \
    G2_SCHED(); \
    st = sn; } while (0)
    BARRIER();
    G2_READ(ax, bx, sm);
    STAMP(4, 1);
    int st = 0;
    for (int kt = 0; kt < NT; kt += 2) {
      G2_ITER(ax, bx, ay, by);
      G2_ITER(ay, by, ax, bx);
    }
    STAMP(4, 2);
    BARRIER();
#pragma unroll
    for (int mi = 0; mi < 8; ++mi)
#pragma unroll
      for (int ni = 0; ni < 4; ++ni) *(f32x4*)(sm + w * 34816 + (mi * 16 + fr) * 272 + (ni * 16 + fq * 4) * 4) = acc[mi][ni];
  }
  uint4 hv[2];
#pragma unroll
  for (int i = 0; i < 2; ++i) { const int id = tid + 512 * i, row = id >> 3, ch = id & 7; hv[i] = *(const uint4*)(H + (size_t)(m0 + row) * D_ + n0 + ch * 8); }
  BARRIER();
  STAMP(4, 3);
#pragma unroll
  for (int i = 0; i < 2; ++i) {
    const int id = tid + 512 * i, row = id >> 3, ch = id & 7;
    f32x4 s0 = (f32x4){0.f, 0.f, 0.f, 0.f}, s1 = s0;
#pragma unroll
    for (int sw = 0; sw < 4; ++sw) { const char* pp = sm + sw * 34816 + row * 272 + ch * 32; s0 += *(const f32x4*)pp; s1 += *(const f32x4*)(pp + 16); }
    uint4 o;
    o.x = pk2(__uint_as_float(hv[i].x << 16) + bfr(s0[0]), __uint_as_float(hv[i].x & 0xffff0000u) + bfr(s0[1]));
    o.y = pk2(__uint_as_float(hv[i].y << 16) + bfr(s0[2]), __uint_as_float(hv[i].y & 0xffff0000u) + bfr(s0[3]));
    o.z = pk2(__uint_as_float(hv[i].z << 16) + bfr(s1[0]), __uint_as_float(hv[i].z & 0xffff0000u) + bfr(s1[1]));
    o.w = pk2(__uint_as_float(hv[i].w << 16) + bfr(s1[2]), __uint_as_float(hv[i].w & 0xffff0000u) + bfr(s1[3]));
    *(uint4*)(C + (size_t)(m0 + row) * D_ + n0 + ch * 8) = o;
  }
  STAMP(4, 4);
}

extern "C" void kernel_launch(void* const* d_in, const int* in_sizes, int n_in, void* d_out, int out_size, void* d_ws, size_t ws_size,
                              hipStream_t stream) {
  const u16* x = (const u16*)d_in[0];
  const u16* wr = (const u16*)d_in[1];
  const u16* wi = (const u16*)d_in[2];
  const u16* w1 = (const u16*)d_in[4];
  const u16* b1 = (const u16*)d_in[5];
  const u16* w2 = (const u16*)d_in[6];
  char* ws = (char*)d_ws;
  float2* T = (float2*)ws;
  u32* Wt = (u32*)(ws + (8u << 20));
  u16* h = (u16*)(ws + (12u << 20));
  u16* ln2 = (u16*)(ws + (16u << 20));
  u16* act = (u16*)(ws + (20u << 20));
  float4* Cs = (float4*)(ws + (36u << 20));
  (void)hipFuncSetAttribute((const void*)k4_gemm1, hipFuncAttributeMaxDynamicSharedMemorySize, G1_STAGE * G1_NST);
  (void)hipFuncSetAttribute((const void*)k5_gemm2, hipFuncAttributeMaxDynamicSharedMemorySize, G2_STAGE * G2_NST);
  k1_ln_rfft<<<512, 512, 0, stream>>>(x, wr, wi, T, Wt, Cs, (const u32*)w1);
  k2_col<<<256, 512, 0, stream>>>(T, Wt, Cs, (const u32*)w2);
  k3_irfft_ln<<<256, 512, 0, stream>>>(T, x, h, ln2);
  k4_gemm1<<<256, 512, G1_STAGE * G1_NST, stream>>>(ln2, w1, b1, act);
  k5_gemm2<<<256, 512, G2_STAGE * G2_NST, stream>>>(act, w2, h, (u16*)d_out);
}
```

```cpp
#include <hip/hip_runtime.h>
#include <hip/hip_bf16.h>

typedef unsigned short u16;
typedef unsigned int u32;
typedef short s16x4 __attribute__((ext_vector_type(4)));
typedef short s16x8 __attribute__((ext_vector_type(8)));
typedef __bf16 bf16x8 __attribute__((ext_vector_type(8)));
typedef float f32x4 __attribute__((ext_vector_type(4)));
typedef u32 u32x2 __attribute__((ext_vector_type(2)));
#define LDS_AS __attribute__((address_space(3)))

#define STAMP(k, i)
constexpr int S_ = 2048, D_ = 1024, F_ = 3584, DF_ = 513;
#define LDACT 3648
constexpr int LA_ = LDACT;

__device__ __forceinline__ float bf2f(u16 v) { return __uint_as_float(((u32)v) << 16); }
__device__ __forceinline__ u16 f2bf(float f) { return __builtin_bit_cast(u16, (__bf16)f); }
__device__ __forceinline__ float bfr(float f) { return bf2f(f2bf(f)); }
__device__ __forceinline__ u32 pk2(float lo, float hi) { return (u32)f2bf(lo) | ((u32)f2bf(hi) << 16); }

__device__ __forceinline__ float2 cadd(float2 a, float2 b) { return make_float2(a.x + b.x, a.y + b.y); }
__device__ __forceinline__ float2 csub(float2 a, float2 b) { return make_float2(a.x - b.x, a.y - b.y); }
__device__ __forceinline__ float2 cmul(float2 a, float2 b) { return make_float2(a.x * b.x - a.y * b.y, a.x * b.y + a.y * b.x); }
__device__ __forceinline__ float2 cmulc(float2 a, float2 b) { return make_float2(a.x * b.x + a.y * b.y, a.y * b.x - a.x * b.y); }
template <int SGN> __device__ __forceinline__ float2 twm(float2 a, float2 w) { return SGN < 0 ? cmul(a, w) : cmulc(a, w); }
template <int SGN> __device__ __forceinline__ float2 muli(float2 a) { return SGN < 0 ? make_float2(a.y, -a.x) : make_float2(-a.y, a.x); }

template <int SGN> __device__ __forceinline__ void dft4(float2& b0, float2& b1, float2& b2, float2& b3) {
  float2 s02 = cadd(b0, b2), d02 = csub(b0, b2), s13 = cadd(b1, b3), d13 = muli<SGN>(csub(b1, b3));
  b0 = cadd(s02, s13); b2 = csub(s02, s13); b1 = cadd(d02, d13); b3 = csub(d02, d13);
}
template <int SGN> __device__ __forceinline__ void dft8(float2 (&a)[8]) {
  const float R = 0.70710678118654752440f;
  float2 e0 = cadd(a[0], a[4]), e1 = cadd(a[1], a[5]), e2 = cadd(a[2], a[6]), e3 = cadd(a[3], a[7]);
  float2 o0 = csub(a[0], a[4]), o1 = csub(a[1], a[5]), o2 = csub(a[2], a[6]), o3 = csub(a[3], a[7]);
  if (SGN < 0) {
    o1 = make_float2((o1.x + o1.y) * R, (o1.y - o1.x) * R);
    o3 = make_float2((o3.y - o3.x) * R, -(o3.x + o3.y) * R);
  } else {
    o1 = make_float2((o1.x - o1.y) * R, (o1.x + o1.y) * R);
    o3 = make_float2(-(o3.x + o3.y) * R, (o3.x - o3.y) * R);
  }
  o2 = muli<SGN>(o2);
  dft4<SGN>(e0, e1, e2, e3);
  dft4<SGN>(o0, o1, o2, o3);
  a[0] = e0; a[2] = e1; a[4] = e2; a[6] = e3;
  a[1] = o0; a[3] = o1; a[5] = o2; a[7] = o3;
}

__device__ __forceinline__ float wave_sum(float v) {
  v += __builtin_bit_cast(float, __builtin_amdgcn_update_dpp(0, __builtin_bit_cast(int, v), 0x111, 0xf, 0xf, true));
  v += __builtin_bit_cast(float, __builtin_amdgcn_update_dpp(0, __builtin_bit_cast(int, v), 0x112, 0xf, 0xf, true));
  v += __builtin_bit_cast(float, __builtin_amdgcn_update_dpp(0, __builtin_bit_cast(int, v), 0x114, 0xf, 0xf, true));
  v += __builtin_bit_cast(float, __builtin_amdgcn_update_dpp(0, __builtin_bit_cast(int, v), 0x118, 0xf, 0xf, true));
  v += __builtin_bit_cast(float, __builtin_amdgcn_update_dpp(0, __builtin_bit_cast(int, v), 0x142, 0xa, 0xf, true));
  v += __builtin_bit_cast(float, __builtin_amdgcn_update_dpp(0, __builtin_bit_cast(int, v), 0x143, 0xc, 0xf, true));
  return __builtin_bit_cast(float, __builtin_amdgcn_readlane(__builtin_bit_cast(int, v), 63));
}
__device__ __forceinline__ float2 cispi_neg(float x) { float s, c; sincospif(x, &s, &c); return make_float2(c, -s); }
__device__ __forceinline__ void pow7(float2 w, float2 (&p)[8]) {
  p[0] = make_float2(1.f, 0.f);
  p[1] = w; p[2] = cmul(w, w); p[3] = cmul(p[2], w); p[4] = cmul(p[2], p[2]);
  p[5] = cmul(p[4], w); p[6] = cmul(p[3], p[3]); p[7] = cmul(p[4], p[3]);
}
#define LGKM0() do { __builtin_amdgcn_s_waitcnt(0xC07F); __builtin_amdgcn_wave_barrier(); } while (0)

template <int SGN> __device__ __forceinline__ void fft512_wave(float2 (&a)[8], float2* bw, int lane) {
  const int l0 = lane & 7, mm = lane >> 3;
  float2 p1[8], p2[8];
  pow7(cispi_neg((float)lane * (1.0f / 256.0f)), p1);
  pow7(cispi_neg((float)l0 * (1.0f / 32.0f)), p2);
  dft8<SGN>(a);
#pragma unroll
  for (int m = 1; m < 8; ++m) a[m] = twm<SGN>(a[m], p1[m]);
#pragma unroll
  for (int m = 0; m < 8; ++m) bw[m * 72 + lane] = a[m];
  LGKM0();
#pragma unroll
  for (int j = 0; j < 8; ++j) a[j] = bw[mm * 72 + l0 + 8 * j];
  LGKM0();
  dft8<SGN>(a);
#pragma unroll
  for (int m = 1; m < 8; ++m) a[m] = twm<SGN>(a[m], p2[m]);
#pragma unroll
  for (int m = 0; m < 8; ++m) bw[l0 * 66 + mm + 8 * m] = a[m];
  LGKM0();
#pragma unroll
  for (int j = 0; j < 8; ++j) a[j] = bw[j * 66 + lane];
  LGKM0();
  dft8<SGN>(a);
}

constexpr int RS_ = 584;

__device__ __forceinline__ int k2_freq(int i, int tau) {
  const int h = tau & 3, m2 = (tau >> 2) & 7, m1 = tau >> 5, e = i >> 2, m4 = i & 3;
  return m1 + 8 * m2 + 64 * (2 * h + e) + 512 * m4;
}
__device__ __forceinline__ float2 rot8(float2 w, int it) {
  const float R = 0.70710678118654752440f;
  return it == 0 ? w : it == 1 ? make_float2((w.x + w.y) * R, (w.y - w.x) * R) : it == 2 ? make_float2(w.y, -w.x) : make_float2((w.y - w.x) * R, -(w.x + w.y) * R);
}

__global__ __launch_bounds__(512) void k1_ln_rfft(const u16* __restrict__ x, const u16* __restrict__ wr, const u16* __restrict__ wi,
                                                  float2* __restrict__ T, u32* __restrict__ Wt, float4* __restrict__ Cs, const u32* __restrict__ pf1, u32* __restrict__ flags) {
  __shared__ __attribute__((aligned(16))) float2 buf[8 * RS_];
  const int tid = threadIdx.x, lane = tid & 63, w = tid >> 6;
  const int r = tid & 7;
  if (blockIdx.x >= 256) {
    const int blk = blockIdx.x - 256;
    if (blk < 16) flags[blk * 512 + tid] = 0u;
    u32 (*wsm)[516] = (u32 (*)[516])buf;
    const int f = k2_freq(blk >> 5, ((blk & 31) << 3) + w);
    const u16* pr = wr + (size_t)f * DF_;
    const u16* pi = wi + (size_t)f * DF_;
    u16 wra[8], wia[8];
#pragma unroll
    for (int j = 0; j < 8; ++j) { wra[j] = pr[lane + 64 * j]; wia[j] = pi[lane + 64 * j]; }
    const u32 pfa = pf1[(blk * 448 + (tid < 448 ? tid : 447)) * 16];
    if (lane == 0) {
      const int fm = (2048 - f) & 2047;
      const float w0r = bf2f(pr[0]), w0i = bf2f(pi[0]), w0mr = bf2f(wr[(size_t)fm * DF_]), w0mi = bf2f(wi[(size_t)fm * DF_]);
      const float wnr = bf2f(pr[512]), wni = bf2f(pi[512]), wnmr = bf2f(wr[(size_t)fm * DF_ + 512]), wnmi = bf2f(wi[(size_t)fm * DF_ + 512]);
      const float2 A = make_float2(0.5f * (w0r + w0mr), 0.5f * (w0i - w0mi));
      const float2 B = make_float2(0.5f * (wnr + wnmr), 0.5f * (wni - wnmi));
      Cs[(blk >> 5) * 256 + ((blk & 31) << 3) + w] = make_float4(0.5f * (A.x + B.x), 0.5f * (A.y + B.y), 0.5f * (A.x - B.x), 0.5f * (A.y - B.y));
    }
#pragma unroll
    for (int j = 0; j < 8; ++j) wsm[w][lane + 64 * j] = (u32)wra[j] | ((u32)wia[j] << 16);
    __syncthreads();
    const int i = blk >> 5, tau0 = (blk & 31) << 3;
#pragma unroll
    for (int it = 0; it < 4; ++it) {
      const int kp = (tid >> 3) + 64 * it;
      u32x2 v; v.x = wsm[r][2 * kp]; v.y = wsm[r][2 * kp + 1];
      *(u32x2*)(Wt + ((size_t)kp * 8 + i) * 512 + 2 * (tau0 + r)) = v;
    }
    asm volatile("" ::"v"(pfa));
    return;
  }
  const int blk = blockIdx.x;
  const int row = blk * 8 + w;
  STAMP(0, 0);
  const u32* xr = (const u32*)(x + (size_t)row * D_);
  u32 raw[8];
#pragma unroll
  for (int j = 0; j < 8; ++j) raw[j] = xr[lane + 64 * j];
  const float2 wpost = cispi_neg((float)(tid >> 3) * (1.0f / 256.0f));
  float2 a[8];
  float sum = 0.f;
#pragma unroll
  for (int j = 0; j < 8; ++j) { a[j] = make_float2(__uint_as_float(raw[j] << 16), __uint_as_float(raw[j] & 0xffff0000u)); sum += a[j].x + a[j].y; }
  const float mean = wave_sum(sum) * (1.0f / 1024.0f);
  float sq = 0.f;
#pragma unroll
  for (int j = 0; j < 8; ++j) { const float dx = a[j].x - mean, dy = a[j].y - mean; sq += dx * dx + dy * dy; }
  const float var = wave_sum(sq) * (1.0f / 1024.0f);
  const float mb = bfr(mean), rden = 1.0f / bfr(sqrtf(bfr(bfr(var) + 1e-5f)));
#pragma unroll
  for (int j = 0; j < 8; ++j) { a[j].x = bfr(bfr(a[j].x - mb) * rden); a[j].y = bfr(bfr(a[j].y - mb) * rden); }
  STAMP(0, 1);
  float2* bw = buf + w * RS_;
  fft512_wave<-1>(a, bw, lane);
#pragma unroll
  for (int m = 0; m < 8; ++m) bw[lane + 64 * m] = a[m];
  __syncthreads();
  STAMP(0, 2);
  const float2* zr = buf + r * RS_;
  const float2 w1 = make_float2(0.99998117528260111f, -0.0061358846491544753f);
#pragma unroll
  for (int it = 0; it < 4; ++it) {
    const int kp = (tid >> 3) + 64 * it;
    const float2 we = rot8(wpost, it);
    float4 o;
#pragma unroll
    for (int e = 0; e < 2; ++e) {
      const int k = 2 * kp + e;
      const float2 zk = zr[k], zm = zr[(512 - k) & 511];
      const float2 sum2 = make_float2(zk.x + zm.x, zk.y - zm.y);
      const float2 dif2 = make_float2(zk.x - zm.x, zk.y + zm.y);
      const float2 t = cmul(dif2, e ? cmul(we, w1) : we);
      float2 X = make_float2(sum2.x + t.y, sum2.y - t.x);
      if (k == 0) X = make_float2(2.f * (zk.x + zk.y), 2.f * (zk.x - zk.y));
      if (e == 0) { o.x = X.x; o.y = X.y; } else { o.z = X.x; o.w = X.y; }
    }
    *(float4*)(T + ((size_t)kp * S_ + blk * 8 + r) * 2) = o;
  }
  STAMP(0, 3);
  STAMP(0, 4);
}

__device__ __forceinline__ int ex3(int g, int m, int v, int c) { return g * 74 + (m * 4 + v) * 2 + c + (m >> 2) * 8; }
__global__ __launch_bounds__(512) void k2_col(float2* __restrict__ T, const u32* __restrict__ Wt, const float4* __restrict__ Cs,
                                              const u32* __restrict__ pf2) {
  __shared__ __attribute__((aligned(16))) float2 exA[4736];
  __shared__ __attribute__((aligned(16))) float2 exB[4736];
  const int t = threadIdx.x, kp = blockIdx.x, c = t & 1, u = t >> 1;
  float2* col = T + (size_t)kp * S_ * 2;
  STAMP(1, 0);
  float2 a[8];
#pragma unroll
  for (int j = 0; j < 8; ++j) a[j] = col[(u + 256 * j) * 2 + c];
  u32 wv[8];
#pragma unroll
  for (int i = 0; i < 8; ++i) wv[i] = Wt[((size_t)kp * 8 + i) * 512 + t];
  float4 cs[8];
  if (kp == 0) {
#pragma unroll
    for (int i = 0; i < 8; ++i) cs[i] = Cs[i * 256 + u];
  }
  const int u0 = u & 31, m1 = u >> 5;
  const int v0 = u & 3, g = u >> 2;
  float2 p1[8], p2[8], p3[8];
  pow7(cispi_neg((float)u * (1.0f / 1024.0f)), p1);
  pow7(cispi_neg((float)u0 * (1.0f / 128.0f)), p2);
  pow7(cispi_neg((float)v0 * (1.0f / 16.0f)), p3);
  STAMP(1, 1);
  dft8<-1>(a);
#pragma unroll
  for (int m = 1; m < 8; ++m) a[m] = cmul(a[m], p1[m]);
#pragma unroll
  for (int m = 0; m < 8; ++m) exA[(m * 256 + u) * 2 + c] = a[m];
  asm volatile("s_waitcnt vmcnt(0)" ::: "memory");
  __syncthreads();
  const int pfi = (kp * 448 + (t < 448 ? t : 447)) * 16;
  const u32 pfb = pf2[pfi];
#pragma unroll
  for (int j = 0; j < 8; ++j) a[j] = exA[(m1 * 256 + u0 + 32 * j) * 2 + c];
  dft8<-1>(a);
#pragma unroll
  for (int m = 1; m < 8; ++m) a[m] = cmul(a[m], p2[m]);
#pragma unroll
  for (int m = 0; m < 8; ++m) exB[((m1 * 8 + m) * 36 + u0) * 2 + c] = a[m];
  __syncthreads();
#pragma unroll
  for (int j = 0; j < 8; ++j) a[j] = exB[(g * 36 + v0 + 4 * j) * 2 + c];
  dft8<-1>(a);
#pragma unroll
  for (int m = 1; m < 8; ++m) a[m] = cmul(a[m], p3[m]);
#pragma unroll
  for (int m = 0; m < 8; ++m) exA[ex3(g, m, v0, c)] = a[m];
  __syncthreads();
#pragma unroll
  for (int e = 0; e < 2; ++e)
#pragma unroll
    for (int v = 0; v < 4; ++v) a[e * 4 + v] = exA[ex3(g, 2 * v0 + e, v, c)];
  dft4<-1>(a[0], a[1], a[2], a[3]);
  dft4<-1>(a[4], a[5], a[6], a[7]);
  STAMP(1, 2);
  if (kp == 0) {
    if (c == 0) {
#pragma unroll
      for (int i = 0; i < 8; ++i) exB[k2_freq(i, u)] = a[i];
    }
    __syncthreads();
    if (c == 0) {
#pragma unroll
      for (int i = 0; i < 8; ++i) {
        const int f = k2_freq(i, u), fm = (2048 - f) & 2047;
        const float2 pm = exB[fm];
        const float2 C1 = make_float2(cs[i].x, cs[i].y), C2 = make_float2(cs[i].z, cs[i].w);
        a[i] = cadd(cmul(a[i], C1), cmul(make_float2(pm.x, -pm.y), C2));
      }
    } else {
#pragma unroll
      for (int i = 0; i < 8; ++i) a[i] = cmul(a[i], make_float2(__uint_as_float(wv[i] << 16), __uint_as_float(wv[i] & 0xffff0000u)));
    }
    __syncthreads();
  } else {
#pragma unroll
    for (int i = 0; i < 8; ++i) a[i] = cmul(a[i], make_float2(__uint_as_float(wv[i] << 16), __uint_as_float(wv[i] & 0xffff0000u)));
  }
  dft4<1>(a[0], a[1], a[2], a[3]);
  dft4<1>(a[4], a[5], a[6], a[7]);
#pragma unroll
  for (int e = 0; e < 2; ++e)
#pragma unroll
    for (int v = 0; v < 4; ++v) exB[ex3(g, 2 * v0 + e, v, c)] = a[e * 4 + v];
  __syncthreads();
#pragma unroll
  for (int m = 0; m < 8; ++m) a[m] = exB[ex3(g, m, v0, c)];
#pragma unroll
  for (int m = 1; m < 8; ++m) a[m] = cmulc(a[m], p3[m]);
  dft8<1>(a);
#pragma unroll
  for (int j = 0; j < 8; ++j) exA[(g * 36 + v0 + 4 * j) * 2 + c] = a[j];
  __syncthreads();
#pragma unroll
  for (int m = 0; m < 8; ++m) a[m] = exA[((m1 * 8 + m) * 36 + u0) * 2 + c];
#pragma unroll
  for (int m = 1; m < 8; ++m) a[m] = cmulc(a[m], p2[m]);
  dft8<1>(a);
#pragma unroll
  for (int j = 0; j < 8; ++j) exB[(m1 * 256 + u0 + 32 * j) * 2 + c] = a[j];
  __syncthreads();
#pragma unroll
  for (int m = 0; m < 8; ++m) a[m] = exB[(m * 256 + u) * 2 + c];
#pragma unroll
  for (int m = 1; m < 8; ++m) a[m] = cmulc(a[m], p1[m]);
  dft8<1>(a);
#pragma unroll
  for (int j = 0; j < 8; ++j) col[(u + 256 * j) * 2 + c] = a[j];
  asm volatile("" ::"v"(pfb));
  STAMP(1, 3);
  STAMP(1, 4);
}

__global__ __launch_bounds__(512) void k3_irfft_ln(const float2* __restrict__ T, const u16* __restrict__ x, u16* __restrict__ hout, u16* __restrict__ ln2) {
  __shared__ __attribute__((aligned(16))) float2 buf[8 * RS_];
  const int tid = threadIdx.x, lane = tid & 63, w = tid >> 6, blk = blockIdx.x;
  const int row = blk * 8 + w;
  STAMP(2, 0);
  {
    const int r = tid & 7;
    float4 v[4];
#pragma unroll
    for (int it = 0; it < 4; ++it) v[it] = *(const float4*)(T + ((size_t)((tid >> 3) + 64 * it) * S_ + blk * 8 + r) * 2);
#pragma unroll
    for (int it = 0; it < 4; ++it) *(float4*)(buf + r * RS_ + 2 * ((tid >> 3) + 64 * it)) = v[it];
  }
  const u32* xr = (const u32*)(x + (size_t)row * D_);
  u32 raw[8];
#pragma unroll
  for (int j = 0; j < 8; ++j) raw[j] = xr[lane + 64 * j];
  const float2 wl = cispi_neg((float)lane * (1.0f / 512.0f));
  __syncthreads();
  STAMP(2, 1);
  float2* bw = buf + w * RS_;
  float2 a[8];
  const float C8 = 0.92387953251128674f, S8 = 0.38268343236508977f;
  const float2 wl2 = cmul(wl, make_float2(C8, -S8));
#pragma unroll
  for (int j = 0; j < 8; ++j) {
    const int k = lane + 64 * j;
    const float2 yk = bw[k], ym = bw[(512 - k) & 511];
    const float2 sum2 = make_float2(yk.x + ym.x, yk.y - ym.y);
    const float2 dif2 = make_float2(yk.x - ym.x, yk.y + ym.y);
    const float2 t = cmulc(dif2, rot8((j & 1) ? wl2 : wl, j >> 1));
    a[j] = make_float2(sum2.x - t.y, sum2.y + t.x);
    if (k == 0) a[j] = make_float2(yk.x + yk.y, yk.x - yk.y);
  }
  LGKM0();
  fft512_wave<1>(a, bw, lane);
  STAMP(2, 2);
  const float scale = 1.0f / 4194304.0f;
  float sum = 0.f;
#pragma unroll
  for (int j = 0; j < 8; ++j) {
    a[j].x = bfr(__uint_as_float(raw[j] << 16) + bfr(a[j].x * scale));
    a[j].y = bfr(__uint_as_float(raw[j] & 0xffff0000u) + bfr(a[j].y * scale));
    sum += a[j].x + a[j].y;
  }
  u32* ho = (u32*)(hout + (size_t)row * D_);
  u32* lo = (u32*)(ln2 + (size_t)row * D_);
#pragma unroll
  for (int j = 0; j < 8; ++j) ho[lane + 64 * j] = pk2(a[j].x, a[j].y);
  const float mean = wave_sum(sum) * (1.0f / 1024.0f);
  float sq = 0.f;
#pragma unroll
  for (int j = 0; j < 8; ++j) { const float dx = a[j].x - mean, dy = a[j].y - mean; sq += dx * dx + dy * dy; }
  const float var = wave_sum(sq) * (1.0f / 1024.0f);
  const float mb = bfr(mean), rden = 1.0f / bfr(sqrtf(bfr(bfr(var) + 1e-5f)));
#pragma unroll
  for (int j = 0; j < 8; ++j) lo[lane + 64 * j] = pk2(bfr(a[j].x - mb) * rden, bfr(a[j].y - mb) * rden);
  STAMP(2, 3);
  STAMP(2, 4);
}

#define WAIT_VM(n) asm volatile("s_waitcnt vmcnt(%0)" ::"n"(n) : "memory")
#define BARRIER() do { asm volatile("s_waitcnt lgkmcnt(0)" ::: "memory"); __builtin_amdgcn_s_barrier(); asm volatile("" ::: "memory"); } while (0)

typedef int i32x4 __attribute__((ext_vector_type(4)));
__device__ __forceinline__ i32x4 mk_rsrc(const void* p, u32 bytes) {
  const unsigned long long a = (unsigned long long)p;
  i32x4 r; r.x = (int)(u32)a; r.y = (int)(u32)(a >> 32); r.z = (int)bytes; r.w = 0x00020000; return r;
}
__device__ __forceinline__ void dma16(i32x4 rs, u32 lds_addr, int voff, int soff) {
  asm volatile("s_mov_b32 m0, %0\n\ts_nop 0\n\tbuffer_load_dwordx4 %1, %2, %3 offen lds" ::"s"(lds_addr), "v"(voff), "s"(rs), "s"(soff) : "memory");
}
__device__ __forceinline__ bf16x8 lds_a(const char* p) { return *(const bf16x8*)p; }
__device__ __forceinline__ bf16x8 lds_tr(const char* p0, const char* p1) {
  s16x4 lo = __builtin_amdgcn_ds_read_tr16_b64_v4i16((LDS_AS s16x4*)p0);
  s16x4 hi = __builtin_amdgcn_ds_read_tr16_b64_v4i16((LDS_AS s16x4*)p1);
  s16x8 v = __builtin_shufflevector(lo, hi, 0, 1, 2, 3, 4, 5, 6, 7);
  return __builtin_bit_cast(bf16x8, v);
}
typedef float f32x2 __attribute__((ext_vector_type(2)));
__device__ __forceinline__ f32x2 mish2(f32x2 h) {
  f32x2 t; t.x = fminf(h.x, 30.f); t.y = fminf(h.y, 30.f);
  t = t * 1.4426950408889634f;
  f32x2 u; u.x = __builtin_amdgcn_exp2f(t.x); u.y = __builtin_amdgcn_exp2f(t.y);
  const f32x2 n = u * (u + 2.f), d = n + 2.f;
  f32x2 r; r.x = __builtin_amdgcn_rcpf(d.x); r.y = __builtin_amdgcn_rcpf(d.y);
  return (h * n) * r;
}

constexpr int G1_STAGE = 16384 + 28672, G1_NST = 3;
__global__ __launch_bounds__(512, 2) void k4_gemm1(const u16* __restrict__ A, const u16* __restrict__ B, const u16* __restrict__ bias, u16* __restrict__ C) {
  extern __shared__ __attribute__((aligned(16))) char sm[];
  const int tid = threadIdx.x, lane = tid & 63, wv = __builtin_amdgcn_readfirstlane(tid >> 6);
  const u32 smbase = (u32)(size_t)(LDS_AS char*)sm;
  const int xcd = blockIdx.x & 7, idx = blockIdx.x >> 3;
  const int tm = (xcd & 1) * 8 + (idx & 7), tn = (xcd >> 1) * 4 + (idx >> 3);
  const int m0 = tm * 128, n0 = tn * 224;
  STAMP(3, 0);
  constexpr int NT = D_ / 64;
  if (wv >= 4) {
    const int w = wv - 4;
    const i32x4 rsA = mk_rsrc(A, S_ * D_ * 2);
    const i32x4 rsB = mk_rsrc(B, D_ * F_ * 2);
    int voA[4], voB[7];
#pragma unroll
    for (int i = 0; i < 4; ++i) { const int row = (w * 4 + i) * 8 + (lane >> 3), cch = (lane & 7) ^ (row & 7); voA[i] = (m0 + row) * (D_ * 2) + cch * 16; }
#pragma unroll
    for (int i = 0; i < 7; ++i) {
      const int L = (w * 7 + i) * 64 + lane, k = L / 28, cc = L % 28, nb = (cc >> 1) ^ ((k >> 3) & 1);
      voB[i] = k * (F_ * 2) + (n0 + nb * 16 + (cc & 1) * 8) * 2;
    }
#define G1_STAGE_LOAD(st, kt) do { const u32 sb_ = smbase + (st) * G1_STAGE; \
    _Pragma("unroll") for (int i = 0; i < 4; ++i) dma16(rsA, sb_ + (w * 4 + i) * 1024, voA[i], (kt) * 128); \
    _Pragma("unroll") for (int i = 0; i < 7; ++i) dma16(rsB, sb_ + 16384 + (w * 7 + i) * 1024, voB[i], (kt) * (64 * F_ * 2)); } while (0)
    G1_STAGE_LOAD(0, 0);
    G1_STAGE_LOAD(1, 1);
    G1_STAGE_LOAD(2, 2);
    WAIT_VM(22);
    BARRIER();
    int st = 0;
    for (int kt = 0; kt < NT; ++kt) {
      if (kt + 2 < NT) WAIT_VM(11); else WAIT_VM(0);
      BARRIER();
      if (kt + 3 < NT) G1_STAGE_LOAD(st, kt + 3);
      st = st == 2 ? 0 : st + 1;
    }
  } else {
    const int wr = wv >> 1, wc = wv & 1, fr = lane & 15, fq = lane >> 4;
    f32x4 acc[4][7];
#pragma unroll
    for (int i = 0; i < 4; ++i)
#pragma unroll
      for (int j = 0; j < 7; ++j) acc[i][j] = (f32x4){0.f, 0.f, 0.f, 0.f};
    const int aoff = (wr * 64 + fr) * 128;
    const int ach0 = ((0 + fq) ^ (fr & 7)) * 16, ach1 = ((4 + fq) ^ (fr & 7)) * 16;
    const int q = (lane & 15) >> 2, p = lane & 3;
    const int boff = 16384 + (8 * fq + q) * 448 + p * 8;
    const int gb = fq & 1;
    bf16x8 ax[4], bx[7], ay[4], by[7];
#define G1_READ(AF, BF, sb, ks) do { \
    _Pragma("unroll") for (int mi = 0; mi < 4; ++mi) AF[mi] = lds_a((sb) + aoff + mi * 2048 + ((ks) ? ach1 : ach0)); \
    _Pragma("unroll") for (int ni = 0; ni < 7; ++ni) { const char* bp = (sb) + boff + (ks) * (32 * 448) + (((wc * 7 + ni) ^ gb) * 32); BF[ni] = lds_tr(bp, bp + 4 * 448); } } while (0)
#define G1_MMA(AF, BF) do { \
    _Pragma("unroll") for (int ni = 0; ni < 7; ++ni) _Pragma("unroll") for (int mi = 0; mi < 4; ++mi) \
      acc[mi][ni] = __builtin_amdgcn_mfma_f32_16x16x32_bf16(BF[ni], AF[mi], acc[mi][ni], 0, 0, 0); } while (0)
#define G1_SCHED() do { _Pragma("unroll") for (int i = 0; i < 18; ++i) { __builtin_amdgcn_sched_group_barrier(0x008, 1, 0); __builtin_amdgcn_sched_group_barrier(0x100, 1, 0); } \
    __builtin_amdgcn_sched_group_barrier(0x008, 10, 0); __builtin_amdgcn_sched_barrier(0); } while (0)
    BARRIER();
    G1_READ(ax, bx, sm, 0);
    STAMP(3, 1);
    int st = 0;
    for (int kt = 0; kt < NT; ++kt) {
      const char* sb = sm + st * G1_STAGE;
      const int sn = st == 2 ? 0 : st + 1;
      __builtin_amdgcn_sched_barrier(0);
      G1_READ(ay, by, sb, 1);
      G1_MMA(ax, bx);
      G1_SCHED();
      BARRIER();
      __builtin_amdgcn_sched_barrier(0);
      G1_READ(ax, bx, sm + sn * G1_STAGE, 0);
      G1_MMA(ay, by);
      G1_SCHED();
      st = sn;
    }
    STAMP(3, 2);
#pragma unroll
    for (int ni = 0; ni < 7; ++ni)
#pragma unroll
      for (int mi = 0; mi < 4; ++mi) *(f32x4*)(sm + (wr * 64 + mi * 16 + fr) * 912 + (wc * 112 + ni * 16 + fq * 4) * 4) = acc[mi][ni];
  }
  BARRIER();
  STAMP(3, 3);
#pragma unroll
  for (int i = 0; i < 7; ++i) {
    const int id = tid + 512 * i, row = id / 28, ch = id % 28;
    const f32x4 v0 = *(const f32x4*)(sm + row * 912 + ch * 32), v1 = *(const f32x4*)(sm + row * 912 + ch * 32 + 16);
    const uint4 bb = *(const uint4*)(bias + n0 + ch * 8);
    const f32x2 r0 = mish2((f32x2){v0[0] + __uint_as_float(bb.x << 16), v0[1] + __uint_as_float(bb.x & 0xffff0000u)});
    const f32x2 r1 = mish2((f32x2){v0[2] + __uint_as_float(bb.y << 16), v0[3] + __uint_as_float(bb.y & 0xffff0000u)});
    const f32x2 r2 = mish2((f32x2){v1[0] + __uint_as_float(bb.z << 16), v1[1] + __uint_as_float(bb.z & 0xffff0000u)});
    const f32x2 r3 = mish2((f32x2){v1[2] + __uint_as_float(bb.w << 16), v1[3] + __uint_as_float(bb.w & 0xffff0000u)});
    uint4 o; o.x = pk2(r0.x, r0.y); o.y = pk2(r1.x, r1.y); o.z = pk2(r2.x, r2.y); o.w = pk2(r3.x, r3.y);
    *(uint4*)((char*)C + (size_t)(m0 + row) * (LA_ * 2) + n0 * 2 + ch * 16) = o;
  }
  STAMP(3, 4);
}

typedef u32 u32x4 __attribute__((ext_vector_type(4)));
constexpr int G2_STAGE = 32768, G2_NST = 4;
__global__ __launch_bounds__(512, 2) void k5_gemm2(const u16* __restrict__ A, const u16* __restrict__ B, const u16* __restrict__ H, u16* __restrict__ C,
                                                   float* P, u32* flags) {
  extern __shared__ __attribute__((aligned(16))) char sm[];
  const int tid = threadIdx.x, lane = tid & 63, wv = __builtin_amdgcn_readfirstlane(tid >> 6);
  const u32 smbase = (u32)(size_t)(LDS_AS char*)sm;
  const int xcd = blockIdx.x & 7, idx = blockIdx.x >> 3, kh = xcd & 1, pair = (xcd >> 1) * 32 + idx;
  const int tm = (xcd >> 1) * 4 + (idx & 3), tn = idx >> 2;
  const int m0 = tm * 128, n0 = tn * 128, k0 = kh * (F_ / 2);
  STAMP(4, 0);
  constexpr int NT = F_ / 2 / 64;
  const __amdgpu_buffer_rsrc_t rp = __builtin_amdgcn_make_buffer_rsrc((void*)P, 0, 8 << 20, 0x00020000);
  const int fr = lane & 15, fq = lane >> 4;
  if (wv >= 4) {
    const int w = wv - 4;
    const i32x4 rsA = mk_rsrc(A, S_ * LA_ * 2);
    const i32x4 rsB = mk_rsrc(B, F_ * D_ * 2);
    int voA[4], voB[4];
#pragma unroll
    for (int i = 0; i < 4; ++i) { const int row = (w * 4 + i) * 8 + (lane >> 3), cch = (lane & 7) ^ (row & 7); voA[i] = (m0 + row) * (LA_ * 2) + k0 * 2 + cch * 16; }
#pragma unroll
    for (int i = 0; i < 4; ++i) {
      const int k = (w * 4 + i) * 4 + (lane >> 4), cc = lane & 15, f = (k & 3) | (((k >> 3) & 1) << 2), nb = (cc >> 1) ^ f;
      voB[i] = (k0 + k) * (D_ * 2) + (n0 + nb * 16 + (cc & 1) * 8) * 2;
    }
#define G2_STAGE_LOAD(st, kt) do { const u32 sb_ = smbase + (st) * G2_STAGE; \
    _Pragma("unroll") for (int i = 0; i < 4; ++i) dma16(rsA, sb_ + (w * 4 + i) * 1024, voA[i], (kt) * 128); \
    _Pragma("unroll") for (int i = 0; i < 4; ++i) dma16(rsB, sb_ + 16384 + (w * 4 + i) * 1024, voB[i], (kt) * (64 * D_ * 2)); } while (0)
    G2_STAGE_LOAD(0, 0);
    G2_STAGE_LOAD(1, 1);
    G2_STAGE_LOAD(2, 2);
    G2_STAGE_LOAD(3, 3);
    WAIT_VM(24);
    BARRIER();
    int st = 0;
    for (int kt = 0; kt < NT; ++kt) {
      if (kt + 3 < NT) WAIT_VM(16); else if (kt + 2 < NT) WAIT_VM(8); else WAIT_VM(0);
      BARRIER();
      if (kt + 4 < NT) G2_STAGE_LOAD(st, kt + 4);
      st = (st + 1) & 3;
    }
    BARRIER();
  } else {
    const int wr = wv >> 1, wc = wv & 1;
    f32x4 acc[4][4];
#pragma unroll
    for (int i = 0; i < 4; ++i)
#pragma unroll
      for (int j = 0; j < 4; ++j) acc[i][j] = (f32x4){0.f, 0.f, 0.f, 0.f};
    const int aoff = (wr * 64 + fr) * 128;
    const int ach0 = ((0 + fq) ^ (fr & 7)) * 16, ach1 = ((4 + fq) ^ (fr & 7)) * 16;
    const int q = (lane & 15) >> 2, p = lane & 3;
    const int bf_ = q | ((fq & 1) << 2);
    const int boff = 16384 + (8 * fq + q) * 256 + p * 8;
    bf16x8 ax[4], bx[4], ay[4], by[4];
#define G2_READ(AF, BF, sb, ks) do { \
    _Pragma("unroll") for (int mi = 0; mi < 4; ++mi) AF[mi] = lds_a((sb) + aoff + mi * 2048 + ((ks) ? ach1 : ach0)); \
    _Pragma("unroll") for (int ni = 0; ni < 4; ++ni) { const char* bp = (sb) + boff + (ks) * 8192 + (((wc * 4 + ni) ^ bf_) * 32); BF[ni] = lds_tr(bp, bp + 1024); } } while (0)
#define G2_MMA(AF, BF) do { \
    _Pragma("unroll") for (int ni = 0; ni < 4; ++ni) _Pragma("unroll") for (int mi = 0; mi < 4; ++mi) \
      acc[mi][ni] = __builtin_amdgcn_mfma_f32_16x16x32_bf16(BF[ni], AF[mi], acc[mi][ni], 0, 0, 0); } while (0)
#define G2_SCHED() do { _Pragma("unroll") for (int i = 0; i < 12; ++i) { __builtin_amdgcn_sched_group_barrier(0x008, 1, 0); __builtin_amdgcn_sched_group_barrier(0x100, 1, 0); } \
    __builtin_amdgcn_sched_group_barrier(0x008, 4, 0); __builtin_amdgcn_sched_barrier(0); } while (0)
    BARRIER();
    G2_READ(ax, bx, sm, 0);
    STAMP(4, 1);
    int st = 0;
    for (int kt = 0; kt < NT; ++kt) {
      const char* sb = sm + st * G2_STAGE;
      const int sn = (st + 1) & 3;
      __builtin_amdgcn_sched_barrier(0);
      G2_READ(ay, by, sb, 1);
      G2_MMA(ax, bx);
      G2_SCHED();
      BARRIER();
      __builtin_amdgcn_sched_barrier(0);
      G2_READ(ax, bx, sm + sn * G2_STAGE, 0);
      G2_MMA(ay, by);
      G2_SCHED();
      st = sn;
    }
    STAMP(4, 2);
    if (wr != kh) {
      const int base = ((pair * 2 + kh) * 2 + wc) * 16384 + lane * 16;
#pragma unroll
      for (int mi = 0; mi < 4; ++mi)
#pragma unroll
        for (int ni = 0; ni < 4; ++ni) __builtin_amdgcn_raw_buffer_store_b128(__builtin_bit_cast(u32x4, acc[mi][ni]), rp, base + (mi * 4 + ni) * 1024, 0, 16);
      WAIT_VM(0);
      if (lane == 0) __hip_atomic_store(flags + ((pair * 2 + kh) * 2 + wc) * 16, 1u, __ATOMIC_RELAXED, __HIP_MEMORY_SCOPE_AGENT);
      BARRIER();
    } else {
      u32* fl = flags + ((pair * 2 + (kh ^ 1)) * 2 + wc) * 16;
      for (int spin = 0; spin < (1 << 24); ++spin) {
        if (__hip_atomic_load(fl, __ATOMIC_RELAXED, __HIP_MEMORY_SCOPE_AGENT) != 0u) break;
        __builtin_amdgcn_s_sleep(1);
      }
      BARRIER();
      const int base = ((pair * 2 + (kh ^ 1)) * 2 + wc) * 16384 + lane * 16;
      u32x4 pv[16];
#pragma unroll
      for (int i = 0; i < 16; ++i) pv[i] = __builtin_amdgcn_raw_buffer_load_b128(rp, base + i * 1024, 0, 16);
#pragma unroll
      for (int mi = 0; mi < 4; ++mi)
#pragma unroll
        for (int ni = 0; ni < 4; ++ni)
          *(f32x4*)(sm + (mi * 16 + fr) * 528 + (wc * 64 + ni * 16 + fq * 4) * 4) = acc[mi][ni] + __builtin_bit_cast(f32x4, pv[mi * 4 + ni]);
    }
  }
  const int mo = m0 + kh * 64;
  uint4 hv[2];
#pragma unroll
  for (int i = 0; i < 2; ++i) { const int id = tid + 512 * i, row = id >> 4, ch = id & 15; hv[i] = *(const uint4*)(H + (size_t)(mo + row) * D_ + n0 + ch * 8); }
  BARRIER();
  STAMP(4, 3);
#pragma unroll
  for (int i = 0; i < 2; ++i) {
    const int id = tid + 512 * i, row = id >> 4, ch = id & 15;
    const f32x4 s0 = *(const f32x4*)(sm + row * 528 + ch * 32), s1 = *(const f32x4*)(sm + row * 528 + ch * 32 + 16);
    uint4 o;
    o.x = pk2(__uint_as_float(hv[i].x << 16) + bfr(s0[0]), __uint_as_float(hv[i].x & 0xffff0000u) + bfr(s0[1]));
    o.y = pk2(__uint_as_float(hv[i].y << 16) + bfr(s0[2]), __uint_as_float(hv[i].y & 0xffff0000u) + bfr(s0[3]));
    o.z = pk2(__uint_as_float(hv[i].z << 16) + bfr(s1[0]), __uint_as_float(hv[i].z & 0xffff0000u) + bfr(s1[1]));
    o.w = pk2(__uint_as_float(hv[i].w << 16) + bfr(s1[2]), __uint_as_float(hv[i].w & 0xffff0000u) + bfr(s1[3]));
    *(uint4*)(C + (size_t)(mo + row) * D_ + n0 + ch * 8) = o;
  }
  STAMP(4, 4);
}

extern "C" void kernel_launch(void* const* d_in, const int* in_sizes, int n_in, void* d_out, int out_size, void* d_ws, size_t ws_size,
                              hipStream_t stream) {
  const u16* x = (const u16*)d_in[0];
  const u16* wr = (const u16*)d_in[1];
  const u16* wi = (const u16*)d_in[2];
  const u16* w1 = (const u16*)d_in[4];
  const u16* b1 = (const u16*)d_in[5];
  const u16* w2 = (const u16*)d_in[6];
  char* ws = (char*)d_ws;
  float2* T = (float2*)ws;
  u32* Wt = (u32*)(ws + (8u << 20));
  u16* h = (u16*)(ws + (12u << 20));
  u16* ln2 = (u16*)(ws + (16u << 20));
  u16* act = (u16*)(ws + (20u << 20));
  float4* Cs = (float4*)(ws + (36u << 20));
  u32* flags = (u32*)(ws + (37u << 20));
  float* P = (float*)(ws + (40u << 20));
  (void)hipFuncSetAttribute((const void*)k4_gemm1, hipFuncAttributeMaxDynamicSharedMemorySize, G1_STAGE * G1_NST);
  (void)hipFuncSetAttribute((const void*)k5_gemm2, hipFuncAttributeMaxDynamicSharedMemorySize, G2_STAGE * G2_NST);
  k1_ln_rfft<<<512, 512, 0, stream>>>(x, wr, wi, T, Wt, Cs, (const u32*)w1, flags);
  k2_col<<<256, 512, 0, stream>>>(T, Wt, Cs, (const u32*)w2);
  k3_irfft_ln<<<256, 512, 0, stream>>>(T, x, h, ln2);
  k4_gemm1<<<256, 512, G1_STAGE * G1_NST, stream>>>(ln2, w1, b1, act);
  k5_gemm2<<<256, 512, G2_STAGE * G2_NST, stream>>>(act, w2, h, (u16*)d_out, P, flags);
}
```

```cpp
#include <hip/hip_runtime.h>
#include <hip/hip_bf16.h>

typedef unsigned short u16;
typedef unsigned int u32;
typedef short s16x4 __attribute__((ext_vector_type(4)));
typedef short s16x8 __attribute__((ext_vector_type(8)));
typedef __bf16 bf16x8 __attribute__((ext_vector_type(8)));
typedef float f32x4 __attribute__((ext_vector_type(4)));
typedef u32 u32x2 __attribute__((ext_vector_type(2)));
#define LDS_AS __attribute__((address_space(3)))

constexpr int S_ = 2048, D_ = 1024, F_ = 3584, DF_ = 513;
constexpr int LA_ = 3648;

__device__ __forceinline__ float bf2f(u16 v) { return __uint_as_float(((u32)v) << 16); }
__device__ __forceinline__ u16 f2bf(float f) { return __builtin_bit_cast(u16, (__bf16)f); }
__device__ __forceinline__ float bfr(float f) { return bf2f(f2bf(f)); }
__device__ __forceinline__ u32 pk2(float lo, float hi) { return (u32)f2bf(lo) | ((u32)f2bf(hi) << 16); }

__device__ __forceinline__ float2 cadd(float2 a, float2 b) { return make_float2(a.x + b.x, a.y + b.y); }
__device__ __forceinline__ float2 csub(float2 a, float2 b) { return make_float2(a.x - b.x, a.y - b.y); }
__device__ __forceinline__ float2 cmul(float2 a, float2 b) { return make_float2(a.x * b.x - a.y * b.y, a.x * b.y + a.y * b.x); }
__device__ __forceinline__ float2 cmulc(float2 a, float2 b) { return make_float2(a.x * b.x + a.y * b.y, a.y * b.x - a.x * b.y); }
template <int SGN> __device__ __forceinline__ float2 twm(float2 a, float2 w) { return SGN < 0 ? cmul(a, w) : cmulc(a, w); }
template <int SGN> __device__ __forceinline__ float2 muli(float2 a) { return SGN < 0 ? make_float2(a.y, -a.x) : make_float2(-a.y, a.x); }

template <int SGN> __device__ __forceinline__ void dft4(float2& b0, float2& b1, float2& b2, float2& b3) {
  float2 s02 = cadd(b0, b2), d02 = csub(b0, b2), s13 = cadd(b1, b3), d13 = muli<SGN>(csub(b1, b3));
  b0 = cadd(s02, s13); b2 = csub(s02, s13); b1 = cadd(d02, d13); b3 = csub(d02, d13);
}
template <int SGN> __device__ __forceinline__ void dft8(float2 (&a)[8]) {
  const float R = 0.70710678118654752440f;
  float2 e0 = cadd(a[0], a[4]), e1 = cadd(a[1], a[5]), e2 = cadd(a[2], a[6]), e3 = cadd(a[3], a[7]);
  float2 o0 = csub(a[0], a[4]), o1 = csub(a[1], a[5]), o2 = csub(a[2], a[6]), o3 = csub(a[3], a[7]);
  if (SGN < 0) {
    o1 = make_float2((o1.x + o1.y) * R, (o1.y - o1.x) * R);
    o3 = make_float2((o3.y - o3.x) * R, -(o3.x + o3.y) * R);
  } else {
    o1 = make_float2((o1.x - o1.y) * R, (o1.x + o1.y) * R);
    o3 = make_float2(-(o3.x + o3.y) * R, (o3.x - o3.y) * R);
  }
  o2 = muli<SGN>(o2);
  dft4<SGN>(e0, e1, e2, e3);
  dft4<SGN>(o0, o1, o2, o3);
  a[0] = e0; a[2] = e1; a[4] = e2; a[6] = e3;
  a[1] = o0; a[3] = o1; a[5] = o2; a[7] = o3;
}

__device__ __forceinline__ float wave_sum(float v) {
  v += __builtin_bit_cast(float, __builtin_amdgcn_update_dpp(0, __builtin_bit_cast(int, v), 0x111, 0xf, 0xf, true));
  v += __builtin_bit_cast(float, __builtin_amdgcn_update_dpp(0, __builtin_bit_cast(int, v), 0x112, 0xf, 0xf, true));
  v += __builtin_bit_cast(float, __builtin_amdgcn_update_dpp(0, __builtin_bit_cast(int, v), 0x114, 0xf, 0xf, true));
  v += __builtin_bit_cast(float, __builtin_amdgcn_update_dpp(0, __builtin_bit_cast(int, v), 0x118, 0xf, 0xf, true));
  v += __builtin_bit_cast(float, __builtin_amdgcn_update_dpp(0, __builtin_bit_cast(int, v), 0x142, 0xa, 0xf, true));
  v += __builtin_bit_cast(float, __builtin_amdgcn_update_dpp(0, __builtin_bit_cast(int, v), 0x143, 0xc, 0xf, true));
  return __builtin_bit_cast(float, __builtin_amdgcn_readlane(__builtin_bit_cast(int, v), 63));
}
__device__ __forceinline__ float2 cispi_neg(float x) { float s, c; sincospif(x, &s, &c); return make_float2(c, -s); }
__device__ __forceinline__ void pow7(float2 w, float2 (&p)[8]) {
  p[0] = make_float2(1.f, 0.f);
  p[1] = w; p[2] = cmul(w, w); p[3] = cmul(p[2], w); p[4] = cmul(p[2], p[2]);
  p[5] = cmul(p[4], w); p[6] = cmul(p[3], p[3]); p[7] = cmul(p[4], p[3]);
}
#define LGKM0() do { __builtin_amdgcn_s_waitcnt(0xC07F); __builtin_amdgcn_wave_barrier(); } while (0)

template <int SGN> __device__ __forceinline__ void fft512_wave(float2 (&a)[8], float2* bw, int lane) {
  const int l0 = lane & 7, mm = lane >> 3;
  float2 p1[8], p2[8];
  pow7(cispi_neg((float)lane * (1.0f / 256.0f)), p1);
  pow7(cispi_neg((float)l0 * (1.0f / 32.0f)), p2);
  dft8<SGN>(a);
#pragma unroll
  for (int m = 1; m < 8; ++m) a[m] = twm<SGN>(a[m], p1[m]);
#pragma unroll
  for (int m = 0; m < 8; ++m) bw[m * 72 + lane] = a[m];
  LGKM0();
#pragma unroll
  for (int j = 0; j < 8; ++j) a[j] = bw[mm * 72 + l0 + 8 * j];
  LGKM0();
  dft8<SGN>(a);
#pragma unroll
  for (int m = 1; m < 8; ++m) a[m] = twm<SGN>(a[m], p2[m]);
#pragma unroll
  for (int m = 0; m < 8; ++m) bw[l0 * 66 + mm + 8 * m] = a[m];
  LGKM0();
#pragma unroll
  for (int j = 0; j < 8; ++j) a[j] = bw[j * 66 + lane];
  LGKM0();
  dft8<SGN>(a);
}

constexpr int RS_ = 584;

__device__ __forceinline__ int k2_freq(int i, int tau) {
  const int h = tau & 3, m2 = (tau >> 2) & 7, m1 = tau >> 5, e = i >> 2, m4 = i & 3;
  return m1 + 8 * m2 + 64 * (2 * h + e) + 512 * m4;
}
__device__ __forceinline__ float2 rot8(float2 w, int it) {
  const float R = 0.70710678118654752440f;
  return it == 0 ? w : it == 1 ? make_float2((w.x + w.y) * R, (w.y - w.x) * R) : it == 2 ? make_float2(w.y, -w.x) : make_float2((w.y - w.x) * R, -(w.x + w.y) * R);
}

constexpr int ST_AUX = 16;
typedef u32 u32x4s __attribute__((ext_vector_type(4)));
typedef u32 u32x2s __attribute__((ext_vector_type(2)));
__device__ __forceinline__ void gst16(void* base, u32 bytes, int off, u32x4s v) { __builtin_amdgcn_raw_buffer_store_b128(v, __builtin_amdgcn_make_buffer_rsrc(base, 0, bytes, 0x00020000), off, 0, ST_AUX); }
__device__ __forceinline__ void gst8(void* base, u32 bytes, int off, u32x2s v) { __builtin_amdgcn_raw_buffer_store_b64(v, __builtin_amdgcn_make_buffer_rsrc(base, 0, bytes, 0x00020000), off, 0, ST_AUX); }
__global__ __launch_bounds__(512) void k1_ln_rfft(const u16* __restrict__ x, const u16* __restrict__ wr, const u16* __restrict__ wi,
                                                  float2* __restrict__ T, u32* __restrict__ Wt, float4* __restrict__ Cs, u32* __restrict__ flags) {
  __shared__ __attribute__((aligned(16))) float2 buf[8 * RS_];
  const int tid = threadIdx.x, lane = tid & 63, w = tid >> 6;
  const int r = tid & 7;
  if (blockIdx.x >= 256) {
    const int blk = blockIdx.x - 256;
    if (blk < 16) flags[blk * 512 + tid] = 0u;
    u32 (*wsm)[516] = (u32 (*)[516])buf;
    const int f = k2_freq(blk >> 5, ((blk & 31) << 3) + w);
    const u16* pr = wr + (size_t)f * DF_;
    const u16* pi = wi + (size_t)f * DF_;
    u16 wra[8], wia[8];
#pragma unroll
    for (int j = 0; j < 8; ++j) { wra[j] = pr[lane + 64 * j]; wia[j] = pi[lane + 64 * j]; }
    if (lane == 0) {
      const int fm = (2048 - f) & 2047;
      const float w0r = bf2f(pr[0]), w0i = bf2f(pi[0]), w0mr = bf2f(wr[(size_t)fm * DF_]), w0mi = bf2f(wi[(size_t)fm * DF_]);
      const float wnr = bf2f(pr[512]), wni = bf2f(pi[512]), wnmr = bf2f(wr[(size_t)fm * DF_ + 512]), wnmi = bf2f(wi[(size_t)fm * DF_ + 512]);
      const float2 A = make_float2(0.5f * (w0r + w0mr), 0.5f * (w0i - w0mi));
      const float2 B = make_float2(0.5f * (wnr + wnmr), 0.5f * (wni - wnmi));
      Cs[(blk >> 5) * 256 + ((blk & 31) << 3) + w] = make_float4(0.5f * (A.x + B.x), 0.5f * (A.y + B.y), 0.5f * (A.x - B.x), 0.5f * (A.y - B.y));
    }
#pragma unroll
    for (int j = 0; j < 8; ++j) wsm[w][lane + 64 * j] = (u32)wra[j] | ((u32)wia[j] << 16);
    __syncthreads();
    const int i = blk >> 5, tau0 = (blk & 31) << 3;
#pragma unroll
    for (int it = 0; it < 4; ++it) {
      const int kp = (tid >> 3) + 64 * it;
      u32x2 v; v.x = wsm[r][2 * kp]; v.y = wsm[r][2 * kp + 1];
      *(u32x2*)(Wt + ((size_t)kp * 8 + i) * 512 + 2 * (tau0 + r)) = v;
    }
    return;
  }
  const int blk = blockIdx.x;
  const int row = blk * 8 + w;
  const u32* xr = (const u32*)(x + (size_t)row * D_);
  u32 raw[8];
#pragma unroll
  for (int j = 0; j < 8; ++j) raw[j] = xr[lane + 64 * j];
  const float2 wpost = cispi_neg((float)(tid >> 3) * (1.0f / 256.0f));
  float2 a[8];
  float sum = 0.f;
#pragma unroll
  for (int j = 0; j < 8; ++j) { a[j] = make_float2(__uint_as_float(raw[j] << 16), __uint_as_float(raw[j] & 0xffff0000u)); sum += a[j].x + a[j].y; }
  const float mean = wave_sum(sum) * (1.0f / 1024.0f);
  float sq = 0.f;
#pragma unroll
  for (int j = 0; j < 8; ++j) { const float dx = a[j].x - mean, dy = a[j].y - mean; sq += dx * dx + dy * dy; }
  const float var = wave_sum(sq) * (1.0f / 1024.0f);
  const float mb = bfr(mean), rden = 1.0f / bfr(sqrtf(bfr(bfr(var) + 1e-5f)));
#pragma unroll
  for (int j = 0; j < 8; ++j) { a[j].x = bfr(bfr(a[j].x - mb) * rden); a[j].y = bfr(bfr(a[j].y - mb) * rden); }
  float2* bw = buf + w * RS_;
  fft512_wave<-1>(a, bw, lane);
#pragma unroll
  for (int m = 0; m < 8; ++m) bw[lane + 64 * m] = a[m];
  __syncthreads();
  const float2* zr = buf + r * RS_;
  const float2 w1 = make_float2(0.99998117528260111f, -0.0061358846491544753f);
#pragma unroll
  for (int it = 0; it < 4; ++it) {
    const int kp = (tid >> 3) + 64 * it;
    const float2 we = rot8(wpost, it);
    float4 o;
#pragma unroll
    for (int e = 0; e < 2; ++e) {
      const int k = 2 * kp + e;
      const float2 zk = zr[k], zm = zr[(512 - k) & 511];
      const float2 sum2 = make_float2(zk.x + zm.x, zk.y - zm.y);
      const float2 dif2 = make_float2(zk.x - zm.x, zk.y + zm.y);
      const float2 t = cmul(dif2, e ? cmul(we, w1) : we);
      float2 X = make_float2(sum2.x + t.y, sum2.y - t.x);
      if (k == 0) X = make_float2(2.f * (zk.x + zk.y), 2.f * (zk.x - zk.y));
      if (e == 0) { o.x = X.x; o.y = X.y; } else { o.z = X.x; o.w = X.y; }
    }
    gst16(T, 8u << 20, (kp * S_ + blk * 8 + r) * 16, __builtin_bit_cast(u32x4s, o));
  }
}

__device__ __forceinline__ int ex3(int g, int m, int v, int c) { return g * 74 + (m * 4 + v) * 2 + c + (m >> 2) * 8; }
__global__ __launch_bounds__(512) void k2_col(float2* __restrict__ T, const u32* __restrict__ Wt, const float4* __restrict__ Cs,
                                              const u32* __restrict__ pf2) {
  __shared__ __attribute__((aligned(16))) float2 exA[4736];
  __shared__ __attribute__((aligned(16))) float2 exB[4736];
  const int t = threadIdx.x, kp = blockIdx.x, c = t & 1, u = t >> 1;
  float2* col = T + (size_t)kp * S_ * 2;
  float2 a[8];
#pragma unroll
  for (int j = 0; j < 8; ++j) a[j] = col[(u + 256 * j) * 2 + c];
  u32 wv[8];
#pragma unroll
  for (int i = 0; i < 8; ++i) wv[i] = Wt[((size_t)kp * 8 + i) * 512 + t];
  float4 cs[8];
  if (kp == 0) {
#pragma unroll
    for (int i = 0; i < 8; ++i) cs[i] = Cs[i * 256 + u];
  }
  const int u0 = u & 31, m1 = u >> 5;
  const int v0 = u & 3, g = u >> 2;
  float2 p1[8], p2[8], p3[8];
  pow7(cispi_neg((float)u * (1.0f / 1024.0f)), p1);
  pow7(cispi_neg((float)u0 * (1.0f / 128.0f)), p2);
  pow7(cispi_neg((float)v0 * (1.0f / 16.0f)), p3);
  dft8<-1>(a);
#pragma unroll
  for (int m = 1; m < 8; ++m) a[m] = cmul(a[m], p1[m]);
#pragma unroll
  for (int m = 0; m < 8; ++m) exA[(m * 256 + u) * 2 + c] = a[m];
  asm volatile("s_waitcnt vmcnt(0)" ::: "memory");
  __syncthreads();
  const int pfi = (kp * 448 + (t < 448 ? t : 447)) * 16;
  const u32 pfb = pf2[pfi];
#pragma unroll
  for (int j = 0; j < 8; ++j) a[j] = exA[(m1 * 256 + u0 + 32 * j) * 2 + c];
  dft8<-1>(a);
#pragma unroll
  for (int m = 1; m < 8; ++m) a[m] = cmul(a[m], p2[m]);
#pragma unroll
  for (int m = 0; m < 8; ++m) exB[((m1 * 8 + m) * 36 + u0) * 2 + c] = a[m];
  __syncthreads();
#pragma unroll
  for (int j = 0; j < 8; ++j) a[j] = exB[(g * 36 + v0 + 4 * j) * 2 + c];
  dft8<-1>(a);
#pragma unroll
  for (int m = 1; m < 8; ++m) a[m] = cmul(a[m], p3[m]);
#pragma unroll
  for (int m = 0; m < 8; ++m) exA[ex3(g, m, v0, c)] = a[m];
  __syncthreads();
#pragma unroll
  for (int e = 0; e < 2; ++e)
#pragma unroll
    for (int v = 0; v < 4; ++v) a[e * 4 + v] = exA[ex3(g, 2 * v0 + e, v, c)];
  dft4<-1>(a[0], a[1], a[2], a[3]);
  dft4<-1>(a[4], a[5], a[6], a[7]);
  if (kp == 0) {
    if (c == 0) {
#pragma unroll
      for (int i = 0; i < 8; ++i) exB[k2_freq(i, u)] = a[i];
    }
    __syncthreads();
    if (c == 0) {
#pragma unroll
      for (int i = 0; i < 8; ++i) {
        const int f = k2_freq(i, u), fm = (2048 - f) & 2047;
        const float2 pm = exB[fm];
        const float2 C1 = make_float2(cs[i].x, cs[i].y), C2 = make_float2(cs[i].z, cs[i].w);
        a[i] = cadd(cmul(a[i], C1), cmul(make_float2(pm.x, -pm.y), C2));
      }
    } else {
#pragma unroll
      for (int i = 0; i < 8; ++i) a[i] = cmul(a[i], make_float2(__uint_as_float(wv[i] << 16), __uint_as_float(wv[i] & 0xffff0000u)));
    }
    __syncthreads();
  } else {
#pragma unroll
    for (int i = 0; i < 8; ++i) a[i] = cmul(a[i], make_float2(__uint_as_float(wv[i] << 16), __uint_as_float(wv[i] & 0xffff0000u)));
  }
  dft4<1>(a[0], a[1], a[2], a[3]);
  dft4<1>(a[4], a[5], a[6], a[7]);
#pragma unroll
  for (int e = 0; e < 2; ++e)
#pragma unroll
    for (int v = 0; v < 4; ++v) exB[ex3(g, 2 * v0 + e, v, c)] = a[e * 4 + v];
  __syncthreads();
#pragma unroll
  for (int m = 0; m < 8; ++m) a[m] = exB[ex3(g, m, v0, c)];
#pragma unroll
  for (int m = 1; m < 8; ++m) a[m] = cmulc(a[m], p3[m]);
  dft8<1>(a);
#pragma unroll
  for (int j = 0; j < 8; ++j) exA[(g * 36 + v0 + 4 * j) * 2 + c] = a[j];
  __syncthreads();
#pragma unroll
  for (int m = 0; m < 8; ++m) a[m] = exA[((m1 * 8 + m) * 36 + u0) * 2 + c];
#pragma unroll
  for (int m = 1; m < 8; ++m) a[m] = cmulc(a[m], p2[m]);
  dft8<1>(a);
#pragma unroll
  for (int j = 0; j < 8; ++j) exB[(m1 * 256 + u0 + 32 * j) * 2 + c] = a[j];
  __syncthreads();
#pragma unroll
  for (int m = 0; m < 8; ++m) a[m] = exB[(m * 256 + u) * 2 + c];
#pragma unroll
  for (int m = 1; m < 8; ++m) a[m] = cmulc(a[m], p1[m]);
  dft8<1>(a);
#pragma unroll
  for (int j = 0; j < 8; ++j) gst8(T, 8u << 20, ((kp * S_ + u + 256 * j) * 2 + c) * 8, __builtin_bit_cast(u32x2s, a[j]));
  asm volatile("" ::"v"(pfb));
}

__global__ __launch_bounds__(512) void k3_irfft_ln(const float2* __restrict__ T, const u16* __restrict__ x, u16* __restrict__ hout, u16* __restrict__ ln2, const u32* __restrict__ pf1) {
  __shared__ __attribute__((aligned(16))) float2 buf[8 * RS_];
  const int tid = threadIdx.x, lane = tid & 63, w = tid >> 6, blk = blockIdx.x;
  const int row = blk * 8 + w;
  {
    const int r = tid & 7;
    float4 v[4];
#pragma unroll
    for (int it = 0; it < 4; ++it) v[it] = *(const float4*)(T + ((size_t)((tid >> 3) + 64 * it) * S_ + blk * 8 + r) * 2);
#pragma unroll
    for (int it = 0; it < 4; ++it) *(float4*)(buf + r * RS_ + 2 * ((tid >> 3) + 64 * it)) = v[it];
  }
  const u32* xr = (const u32*)(x + (size_t)row * D_);
  u32 raw[8];
#pragma unroll
  for (int j = 0; j < 8; ++j) raw[j] = xr[lane + 64 * j];
  const float2 wl = cispi_neg((float)lane * (1.0f / 512.0f));
  __syncthreads();
  const u32 pfa = pf1[(blk * 448 + (tid < 448 ? tid : 447)) * 16];
  float2* bw = buf + w * RS_;
  float2 a[8];
  const float C8 = 0.92387953251128674f, S8 = 0.38268343236508977f;
  const float2 wl2 = cmul(wl, make_float2(C8, -S8));
#pragma unroll
  for (int j = 0; j < 8; ++j) {
    const int k = lane + 64 * j;
    const float2 yk = bw[k], ym = bw[(512 - k) & 511];
    const float2 sum2 = make_float2(yk.x + ym.x, yk.y - ym.y);
    const float2 dif2 = make_float2(yk.x - ym.x, yk.y + ym.y);
    const float2 t = cmulc(dif2, rot8((j & 1) ? wl2 : wl, j >> 1));
    a[j] = make_float2(sum2.x - t.y, sum2.y + t.x);
    if (k == 0) a[j] = make_float2(yk.x + yk.y, yk.x - yk.y);
  }
  LGKM0();
  fft512_wave<1>(a, bw, lane);
  const float scale = 1.0f / 4194304.0f;
  float sum = 0.f;
#pragma unroll
  for (int j = 0; j < 8; ++j) {
    a[j].x = bfr(__uint_as_float(raw[j] << 16) + bfr(a[j].x * scale));
    a[j].y = bfr(__uint_as_float(raw[j] & 0xffff0000u) + bfr(a[j].y * scale));
    sum += a[j].x + a[j].y;
  }
  u32* wb = (u32*)bw;
#pragma unroll
  for (int j = 0; j < 8; ++j) wb[lane + 64 * j] = pk2(a[j].x, a[j].y);
  const float mean = wave_sum(sum) * (1.0f / 1024.0f);
  float sq = 0.f;
#pragma unroll
  for (int j = 0; j < 8; ++j) { const float dx = a[j].x - mean, dy = a[j].y - mean; sq += dx * dx + dy * dy; }
  const float var = wave_sum(sq) * (1.0f / 1024.0f);
  const float mb = bfr(mean), rden = 1.0f / bfr(sqrtf(bfr(bfr(var) + 1e-5f)));
#pragma unroll
  for (int j = 0; j < 8; ++j) wb[512 + lane + 64 * j] = pk2(bfr(a[j].x - mb) * rden, bfr(a[j].y - mb) * rden);
  LGKM0();
#pragma unroll
  for (int i = 0; i < 2; ++i) {
    gst16(ln2, S_ * D_ * 2, row * 2048 + (lane + 64 * i) * 16, *(const u32x4s*)(wb + 512 + (lane + 64 * i) * 4));
    gst16(hout, S_ * D_ * 2, row * 2048 + (lane + 64 * i) * 16, *(const u32x4s*)(wb + (lane + 64 * i) * 4));
  }
  asm volatile("" ::"v"(pfa));
}

#define WAIT_VM(n) asm volatile("s_waitcnt vmcnt(%0)" ::"n"(n) : "memory")
#define BARRIER() do { asm volatile("s_waitcnt lgkmcnt(0)" ::: "memory"); __builtin_amdgcn_s_barrier(); asm volatile("" ::: "memory"); } while (0)

typedef int i32x4 __attribute__((ext_vector_type(4)));
__device__ __forceinline__ i32x4 mk_rsrc(const void* p, u32 bytes) {
  const unsigned long long a = (unsigned long long)p;
  i32x4 r; r.x = (int)(u32)a; r.y = (int)(u32)(a >> 32); r.z = (int)bytes; r.w = 0x00020000; return r;
}
__device__ __forceinline__ void dma16(i32x4 rs, u32 lds_addr, int voff, int soff) {
  asm volatile("s_mov_b32 m0, %0\n\ts_nop 0\n\tbuffer_load_dwordx4 %1, %2, %3 offen lds" ::"s"(lds_addr), "v"(voff), "s"(rs), "s"(soff) : "memory");
}
__device__ __forceinline__ bf16x8 lds_a(const char* p) { return *(const bf16x8*)p; }
__device__ __forceinline__ bf16x8 lds_tr(const char* p0, const char* p1) {
  s16x4 lo = __builtin_amdgcn_ds_read_tr16_b64_v4i16((LDS_AS s16x4*)p0);
  s16x4 hi = __builtin_amdgcn_ds_read_tr16_b64_v4i16((LDS_AS s16x4*)p1);
  s16x8 v = __builtin_shufflevector(lo, hi, 0, 1, 2, 3, 4, 5, 6, 7);
  return __builtin_bit_cast(bf16x8, v);
}
typedef float f32x2 __attribute__((ext_vector_type(2)));
__device__ __forceinline__ f32x2 mish2(f32x2 h) {
  f32x2 t; t.x = fminf(h.x, 30.f); t.y = fminf(h.y, 30.f);
  t = t * 1.4426950408889634f;
  f32x2 u; u.x = __builtin_amdgcn_exp2f(t.x); u.y = __builtin_amdgcn_exp2f(t.y);
  const f32x2 n = u * (u + 2.f), d = n + 2.f;
  f32x2 r; r.x = __builtin_amdgcn_rcpf(d.x); r.y = __builtin_amdgcn_rcpf(d.y);
  return (h * n) * r;
}

constexpr int G1_STAGE = 16384 + 28672, G1_NST = 3;
__global__ __launch_bounds__(512, 2) void k4_gemm1(const u16* __restrict__ A, const u16* __restrict__ B, const u16* __restrict__ bias, u16* __restrict__ C) {
  extern __shared__ __attribute__((aligned(16))) char sm[];
  const int tid = threadIdx.x, lane = tid & 63, wv = __builtin_amdgcn_readfirstlane(tid >> 6);
  const u32 smbase = (u32)(size_t)(LDS_AS char*)sm;
  const int xcd = blockIdx.x & 7, idx = blockIdx.x >> 3;
  const int tm = (xcd & 1) * 8 + (idx & 7), tn = (xcd >> 1) * 4 + (idx >> 3);
  const int m0 = tm * 128, n0 = tn * 224;
  constexpr int NT = D_ / 64;
  if (wv >= 4) {
    const int w = wv - 4;
    const i32x4 rsA = mk_rsrc(A, S_ * D_ * 2);
    const i32x4 rsB = mk_rsrc(B, D_ * F_ * 2);
    int voA[4], voB[7];
#pragma unroll
    for (int i = 0; i < 4; ++i) { const int row = (w * 4 + i) * 8 + (lane >> 3), cch = (lane & 7) ^ (row & 7); voA[i] = (m0 + row) * (D_ * 2) + cch * 16; }
#pragma unroll
    for (int i = 0; i < 7; ++i) {
      const int L = (w * 7 + i) * 64 + lane, k = L / 28, cc = L % 28, nb = (cc >> 1) ^ ((k >> 3) & 1);
      voB[i] = k * (F_ * 2) + (n0 + nb * 16 + (cc & 1) * 8) * 2;
    }
#define G1_STAGE_LOAD(st, kt) do { const u32 sb_ = smbase + (st) * G1_STAGE; \
    _Pragma("unroll") for (int i = 0; i < 4; ++i) dma16(rsA, sb_ + (w * 4 + i) * 1024, voA[i], (kt) * 128); \
    _Pragma("unroll") for (int i = 0; i < 7; ++i) dma16(rsB, sb_ + 16384 + (w * 7 + i) * 1024, voB[i], (kt) * (64 * F_ * 2)); } while (0)
    G1_STAGE_LOAD(0, 0);
    G1_STAGE_LOAD(1, 1);
    G1_STAGE_LOAD(2, 2);
    WAIT_VM(22);
    BARRIER();
    int st = 0;
    for (int kt = 0; kt < NT; ++kt) {
      if (kt + 2 < NT) WAIT_VM(11); else WAIT_VM(0);
      BARRIER();
      if (kt + 3 < NT) G1_STAGE_LOAD(st, kt + 3);
      st = st == 2 ? 0 : st + 1;
    }
  } else {
    const int wr = wv >> 1, wc = wv & 1, fr = lane & 15, fq = lane >> 4;
    f32x4 acc[4][7];
#pragma unroll
    for (int i = 0; i < 4; ++i)
#pragma unroll
      for (int j = 0; j < 7; ++j) acc[i][j] = (f32x4){0.f, 0.f, 0.f, 0.f};
    const int aoff = (wr * 64 + fr) * 128;
    const int ach0 = ((0 + fq) ^ (fr & 7)) * 16, ach1 = ((4 + fq) ^ (fr & 7)) * 16;
    const int q = (lane & 15) >> 2, p = lane & 3;
    const int boff = 16384 + (8 * fq + q) * 448 + p * 8;
    const int gb = fq & 1;
    bf16x8 ax[4], bx[7], ay[4], by[7];
#define G1_READ(AF, BF, sb, ks) do { \
    _Pragma("unroll") for (int mi = 0; mi < 4; ++mi) AF[mi] = lds_a((sb) + aoff + mi * 2048 + ((ks) ? ach1 : ach0)); \
    _Pragma("unroll") for (int ni = 0; ni < 7; ++ni) { const char* bp = (sb) + boff + (ks) * (32 * 448) + (((wc * 7 + ni) ^ gb) * 32); BF[ni] = lds_tr(bp, bp + 4 * 448); } } while (0)
#define G1_MMA(AF, BF) do { \
    _Pragma("unroll") for (int ni = 0; ni < 7; ++ni) _Pragma("unroll") for (int mi = 0; mi < 4; ++mi) \
      acc[mi][ni] = __builtin_amdgcn_mfma_f32_16x16x32_bf16(BF[ni], AF[mi], acc[mi][ni], 0, 0, 0); } while (0)
#define G1_SCHED() do { _Pragma("unroll") for (int i = 0; i < 18; ++i) { __builtin_amdgcn_sched_group_barrier(0x008, 1, 0); __builtin_amdgcn_sched_group_barrier(0x100, 1, 0); } \
    __builtin_amdgcn_sched_group_barrier(0x008, 10, 0); __builtin_amdgcn_sched_barrier(0); } while (0)
    BARRIER();
    G1_READ(ax, bx, sm, 0);
    int st = 0;
    for (int kt = 0; kt < NT; ++kt) {
      const char* sb = sm + st * G1_STAGE;
      const int sn = st == 2 ? 0 : st + 1;
      __builtin_amdgcn_sched_barrier(0);
      G1_READ(ay, by, sb, 1);
      G1_MMA(ax, bx);
      G1_SCHED();
      BARRIER();
      __builtin_amdgcn_sched_barrier(0);
      G1_READ(ax, bx, sm + sn * G1_STAGE, 0);
      G1_MMA(ay, by);
      G1_SCHED();
      st = sn;
    }
#pragma unroll
    for (int ni = 0; ni < 7; ++ni)
#pragma unroll
      for (int mi = 0; mi < 4; ++mi) *(f32x4*)(sm + (wr * 64 + mi * 16 + fr) * 912 + (wc * 112 + ni * 16 + fq * 4) * 4) = acc[mi][ni];
  }
  BARRIER();
#pragma unroll
  for (int i = 0; i < 7; ++i) {
    const int id = tid + 512 * i, row = id / 28, ch = id % 28;
    const f32x4 v0 = *(const f32x4*)(sm + row * 912 + ch * 32), v1 = *(const f32x4*)(sm + row * 912 + ch * 32 + 16);
    const uint4 bb = *(const uint4*)(bias + n0 + ch * 8);
    const f32x2 r0 = mish2((f32x2){v0[0] + __uint_as_float(bb.x << 16), v0[1] + __uint_as_float(bb.x & 0xffff0000u)});
    const f32x2 r1 = mish2((f32x2){v0[2] + __uint_as_float(bb.y << 16), v0[3] + __uint_as_float(bb.y & 0xffff0000u)});
    const f32x2 r2 = mish2((f32x2){v1[0] + __uint_as_float(bb.z << 16), v1[1] + __uint_as_float(bb.z & 0xffff0000u)});
    const f32x2 r3 = mish2((f32x2){v1[2] + __uint_as_float(bb.w << 16), v1[3] + __uint_as_float(bb.w & 0xffff0000u)});
    uint4 o; o.x = pk2(r0.x, r0.y); o.y = pk2(r1.x, r1.y); o.z = pk2(r2.x, r2.y); o.w = pk2(r3.x, r3.y);
    gst16(C, S_ * LA_ * 2, (m0 + row) * (LA_ * 2) + n0 * 2 + ch * 16, __builtin_bit_cast(u32x4s, o));
  }
}

typedef u32 u32x4 __attribute__((ext_vector_type(4)));
constexpr int G2_STAGE = 32768, G2_NST = 4;
__global__ __launch_bounds__(512, 2) void k5_gemm2(const u16* __restrict__ A, const u16* __restrict__ B, const u16* __restrict__ H, u16* __restrict__ C,
                                                   float* P, u32* flags) {
  extern __shared__ __attribute__((aligned(16))) char sm[];
  const int tid = threadIdx.x, lane = tid & 63, wv = __builtin_amdgcn_readfirstlane(tid >> 6);
  const u32 smbase = (u32)(size_t)(LDS_AS char*)sm;
  const int xcd = blockIdx.x & 7, idx = blockIdx.x >> 3, kh = xcd & 1, pair = (xcd >> 1) * 32 + idx;
  const int tm = (xcd >> 1) * 4 + (idx & 3), tn = idx >> 2;
  const int m0 = tm * 128, n0 = tn * 128, k0 = kh * (F_ / 2);
  constexpr int NT = F_ / 2 / 64;
  const __amdgpu_buffer_rsrc_t rp = __builtin_amdgcn_make_buffer_rsrc((void*)P, 0, 8 << 20, 0x00020000);
  const int fr = lane & 15, fq = lane >> 4;
  if (wv >= 4) {
    const int w = wv - 4;
    const i32x4 rsA = mk_rsrc(A, S_ * LA_ * 2);
    const i32x4 rsB = mk_rsrc(B, F_ * D_ * 2);
    int voA[4], voB[4];
#pragma unroll
    for (int i = 0; i < 4; ++i) { const int row = (w * 4 + i) * 8 + (lane >> 3), cch = (lane & 7) ^ (row & 7); voA[i] = (m0 + row) * (LA_ * 2) + k0 * 2 + cch * 16; }
#pragma unroll
    for (int i = 0; i < 4; ++i) {
      const int k = (w * 4 + i) * 4 + (lane >> 4), cc = lane & 15, f = (k & 3) | (((k >> 3) & 1) << 2), nb = (cc >> 1) ^ f;
      voB[i] = (k0 + k) * (D_ * 2) + (n0 + nb * 16 + (cc & 1) * 8) * 2;
    }
#define G2_STAGE_LOAD(st, kt) do { const u32 sb_ = smbase + (st) * G2_STAGE; \
    _Pragma("unroll") for (int i = 0; i < 4; ++i) dma16(rsA, sb_ + (w * 4 + i) * 1024, voA[i], (kt) * 128); \
    _Pragma("unroll") for (int i = 0; i < 4; ++i) dma16(rsB, sb_ + 16384 + (w * 4 + i) * 1024, voB[i], (kt) * (64 * D_ * 2)); } while (0)
    G2_STAGE_LOAD(0, 0);
    G2_STAGE_LOAD(1, 1);
    G2_STAGE_LOAD(2, 2);
    G2_STAGE_LOAD(3, 3);
    WAIT_VM(24);
    BARRIER();
    int st = 0;
    for (int kt = 0; kt < NT; ++kt) {
      if (kt + 3 < NT) WAIT_VM(16); else if (kt + 2 < NT) WAIT_VM(8); else WAIT_VM(0);
      BARRIER();
      if (kt + 4 < NT) G2_STAGE_LOAD(st, kt + 4);
      st = (st + 1) & 3;
    }
    BARRIER();
  } else {
    const int wr = wv >> 1, wc = wv & 1;
    f32x4 acc[4][4];
#pragma unroll
    for (int i = 0; i < 4; ++i)
#pragma unroll
      for (int j = 0; j < 4; ++j) acc[i][j] = (f32x4){0.f, 0.f, 0.f, 0.f};
    const int aoff = (wr * 64 + fr) * 128;
    const int ach0 = ((0 + fq) ^ (fr & 7)) * 16, ach1 = ((4 + fq) ^ (fr & 7)) * 16;
    const int q = (lane & 15) >> 2, p = lane & 3;
    const int bf_ = q | ((fq & 1) << 2);
    const int boff = 16384 + (8 * fq + q) * 256 + p * 8;
    bf16x8 ax[4], bx[4], ay[4], by[4];
#define G2_READ(AF, BF, sb, ks) do { \
    _Pragma("unroll") for (int mi = 0; mi < 4; ++mi) AF[mi] = lds_a((sb) + aoff + mi * 2048 + ((ks) ? ach1 : ach0)); \
    _Pragma("unroll") for (int ni = 0; ni < 4; ++ni) { const char* bp = (sb) + boff + (ks) * 8192 + (((wc * 4 + ni) ^ bf_) * 32); BF[ni] = lds_tr(bp, bp + 1024); } } while (0)
#define G2_MMA(AF, BF) do { \
    _Pragma("unroll") for (int ni = 0; ni < 4; ++ni) _Pragma("unroll") for (int mi = 0; mi < 4; ++mi) \
      acc[mi][ni] = __builtin_amdgcn_mfma_f32_16x16x32_bf16(BF[ni], AF[mi], acc[mi][ni], 0, 0, 0); } while (0)
#define G2_SCHED() do { _Pragma("unroll") for (int i = 0; i < 12; ++i) { __builtin_amdgcn_sched_group_barrier(0x008, 1, 0); __builtin_amdgcn_sched_group_barrier(0x100, 1, 0); } \
    __builtin_amdgcn_sched_group_barrier(0x008, 4, 0); __builtin_amdgcn_sched_barrier(0); } while (0)
    BARRIER();
    G2_READ(ax, bx, sm, 0);
    int st = 0;
    for (int kt = 0; kt < NT; ++kt) {
      const char* sb = sm + st * G2_STAGE;
      const int sn = (st + 1) & 3;
      __builtin_amdgcn_sched_barrier(0);
      G2_READ(ay, by, sb, 1);
      G2_MMA(ax, bx);
      G2_SCHED();
      BARRIER();
      __builtin_amdgcn_sched_barrier(0);
      G2_READ(ax, bx, sm + sn * G2_STAGE, 0);
      G2_MMA(ay, by);
      G2_SCHED();
      st = sn;
    }
    if (wr != kh) {
      const int base = ((pair * 2 + kh) * 2 + wc) * 16384 + lane * 16;
#pragma unroll
      for (int mi = 0; mi < 4; ++mi)
#pragma unroll
        for (int ni = 0; ni < 4; ++ni) __builtin_amdgcn_raw_buffer_store_b128(__builtin_bit_cast(u32x4, acc[mi][ni]), rp, base + (mi * 4 + ni) * 1024, 0, 16);
      WAIT_VM(0);
      if (lane == 0) __hip_atomic_store(flags + ((pair * 2 + kh) * 2 + wc) * 16, 1u, __ATOMIC_RELAXED, __HIP_MEMORY_SCOPE_AGENT);
      BARRIER();
    } else {
      u32* fl = flags + ((pair * 2 + (kh ^ 1)) * 2 + wc) * 16;
      for (int spin = 0; spin < (1 << 20); ++spin) {
        if (__hip_atomic_load(fl, __ATOMIC_RELAXED, __HIP_MEMORY_SCOPE_AGENT) != 0u) break;
        __builtin_amdgcn_s_sleep(1);
      }
      BARRIER();
      const int base = ((pair * 2 + (kh ^ 1)) * 2 + wc) * 16384 + lane * 16;
      u32x4 pv[16];
#pragma unroll
      for (int i = 0; i < 16; ++i) pv[i] = __builtin_amdgcn_raw_buffer_load_b128(rp, base + i * 1024, 0, 16);
#pragma unroll
      for (int mi = 0; mi < 4; ++mi)
#pragma unroll
        for (int ni = 0; ni < 4; ++ni)
          *(f32x4*)(sm + (mi * 16 + fr) * 528 + (wc * 64 + ni * 16 + fq * 4) * 4) = acc[mi][ni] + __builtin_bit_cast(f32x4, pv[mi * 4 + ni]);
    }
  }
  const int mo = m0 + kh * 64;
  uint4 hv[2];
#pragma unroll
  for (int i = 0; i < 2; ++i) { const int id = tid + 512 * i, row = id >> 4, ch = id & 15; hv[i] = *(const uint4*)(H + (size_t)(mo + row) * D_ + n0 + ch * 8); }
  BARRIER();
#pragma unroll
  for (int i = 0; i < 2; ++i) {
    const int id = tid + 512 * i, row = id >> 4, ch = id & 15;
    const f32x4 s0 = *(const f32x4*)(sm + row * 528 + ch * 32), s1 = *(const f32x4*)(sm + row * 528 + ch * 32 + 16);
    uint4 o;
    o.x = pk2(__uint_as_float(hv[i].x << 16) + bfr(s0[0]), __uint_as_float(hv[i].x & 0xffff0000u) + bfr(s0[1]));
    o.y = pk2(__uint_as_float(hv[i].y << 16) + bfr(s0[2]), __uint_as_float(hv[i].y & 0xffff0000u) + bfr(s0[3]));
    o.z = pk2(__uint_as_float(hv[i].z << 16) + bfr(s1[0]), __uint_as_float(hv[i].z & 0xffff0000u) + bfr(s1[1]));
    o.w = pk2(__uint_as_float(hv[i].w << 16) + bfr(s1[2]), __uint_as_float(hv[i].w & 0xffff0000u) + bfr(s1[3]));
    *(uint4*)(C + (size_t)(mo + row) * D_ + n0 + ch * 8) = o;
  }
}

extern "C" void kernel_launch(void* const* d_in, const int* in_sizes, int n_in, void* d_out, int out_size, void* d_ws, size_t ws_size,
                              hipStream_t stream) {
  const u16* x = (const u16*)d_in[0];
  const u16* wr = (const u16*)d_in[1];
  const u16* wi = (const u16*)d_in[2];
  const u16* w1 = (const u16*)d_in[4];
  const u16* b1 = (const u16*)d_in[5];
  const u16* w2 = (const u16*)d_in[6];
  char* ws = (char*)d_ws;
  float2* T = (float2*)ws;
  u32* Wt = (u32*)(ws + (8u << 20));
  u16* h = (u16*)(ws + (12u << 20));
  u16* ln2 = (u16*)(ws + (16u << 20));
  u16* act = (u16*)(ws + (20u << 20));
  float4* Cs = (float4*)(ws + (36u << 20));
  u32* flags = (u32*)(ws + (37u << 20));
  float* P = (float*)(ws + (40u << 20));
  (void)hipFuncSetAttribute((const void*)k4_gemm1, hipFuncAttributeMaxDynamicSharedMemorySize, G1_STAGE * G1_NST);
  (void)hipFuncSetAttribute((const void*)k5_gemm2, hipFuncAttributeMaxDynamicSharedMemorySize, G2_STAGE * G2_NST);
  k1_ln_rfft<<<512, 512, 0, stream>>>(x, wr, wi, T, Wt, Cs, flags);
  k2_col<<<256, 512, 0, stream>>>(T, Wt, Cs, (const u32*)w2);
  k3_irfft_ln<<<256, 512, 0, stream>>>(T, x, h, ln2, (const u32*)w1);
  k4_gemm1<<<256, 512, G1_STAGE * G1_NST, stream>>>(ln2, w1, b1, act);
  k5_gemm2<<<256, 512, G2_STAGE * G2_NST, stream>>>(act, w2, h, (u16*)d_out, P, flags);
}
```

```cpp
#include <hip/hip_runtime.h>
#include <hip/hip_bf16.h>

typedef unsigned short u16;
typedef unsigned int u32;
typedef short s16x4 __attribute__((ext_vector_type(4)));
typedef short s16x8 __attribute__((ext_vector_type(8)));
typedef __bf16 bf16x8 __attribute__((ext_vector_type(8)));
typedef float f32x4 __attribute__((ext_vector_type(4)));
typedef u32 u32x2 __attribute__((ext_vector_type(2)));
#define LDS_AS __attribute__((address_space(3)))

constexpr int S_ = 2048, D_ = 1024, F_ = 3584, DF_ = 513;
constexpr int LA_ = 3648;

__device__ __forceinline__ float bf2f(u16 v) { return __uint_as_float(((u32)v) << 16); }
__device__ __forceinline__ u16 f2bf(float f) { return __builtin_bit_cast(u16, (__bf16)f); }
__device__ __forceinline__ float bfr(float f) { return bf2f(f2bf(f)); }
__device__ __forceinline__ u32 pk2(float lo, float hi) { return (u32)f2bf(lo) | ((u32)f2bf(hi) << 16); }

__device__ __forceinline__ float2 cadd(float2 a, float2 b) { return make_float2(a.x + b.x, a.y + b.y); }
__device__ __forceinline__ float2 csub(float2 a, float2 b) { return make_float2(a.x - b.x, a.y - b.y); }
__device__ __forceinline__ float2 cmul(float2 a, float2 b) { return make_float2(a.x * b.x - a.y * b.y, a.x * b.y + a.y * b.x); }
__device__ __forceinline__ float2 cmulc(float2 a, float2 b) { return make_float2(a.x * b.x + a.y * b.y, a.y * b.x - a.x * b.y); }
template <int SGN> __device__ __forceinline__ float2 twm(float2 a, float2 w) { return SGN < 0 ? cmul(a, w) : cmulc(a, w); }
template <int SGN> __device__ __forceinline__ float2 muli(float2 a) { return SGN < 0 ? make_float2(a.y, -a.x) : make_float2(-a.y, a.x); }

template <int SGN> __device__ __forceinline__ void dft4(float2& b0, float2& b1, float2& b2, float2& b3) {
  float2 s02 = cadd(b0, b2), d02 = csub(b0, b2), s13 = cadd(b1, b3), d13 = muli<SGN>(csub(b1, b3));
  b0 = cadd(s02, s13); b2 = csub(s02, s13); b1 = cadd(d02, d13); b3 = csub(d02, d13);
}
template <int SGN> __device__ __forceinline__ void dft8(float2 (&a)[8]) {
  const float R = 0.70710678118654752440f;
  float2 e0 = cadd(a[0], a[4]), e1 = cadd(a[1], a[5]), e2 = cadd(a[2], a[6]), e3 = cadd(a[3], a[7]);
  float2 o0 = csub(a[0], a[4]), o1 = csub(a[1], a[5]), o2 = csub(a[2], a[6]), o3 = csub(a[3], a[7]);
  if (SGN < 0) {
    o1 = make_float2((o1.x + o1.y) * R, (o1.y - o1.x) * R);
    o3 = make_float2((o3.y - o3.x) * R, -(o3.x + o3.y) * R);
  } else {
    o1 = make_float2((o1.x - o1.y) * R, (o1.x + o1.y) * R);
    o3 = make_float2(-(o3.x + o3.y) * R, (o3.x - o3.y) * R);
  }
  o2 = muli<SGN>(o2);
  dft4<SGN>(e0, e1, e2, e3);
  dft4<SGN>(o0, o1, o2, o3);
  a[0] = e0; a[2] = e1; a[4] = e2; a[6] = e3;
  a[1] = o0; a[3] = o1; a[5] = o2; a[7] = o3;
}

__device__ __forceinline__ float wave_sum(float v) {
  v += __builtin_bit_cast(float, __builtin_amdgcn_update_dpp(0, __builtin_bit_cast(int, v), 0x111, 0xf, 0xf, true));
  v += __builtin_bit_cast(float, __builtin_amdgcn_update_dpp(0, __builtin_bit_cast(int, v), 0x112, 0xf, 0xf, true));
  v += __builtin_bit_cast(float, __builtin_amdgcn_update_dpp(0, __builtin_bit_cast(int, v), 0x114, 0xf, 0xf, true));
  v += __builtin_bit_cast(float, __builtin_amdgcn_update_dpp(0, __builtin_bit_cast(int, v), 0x118, 0xf, 0xf, true));
  v += __builtin_bit_cast(float, __builtin_amdgcn_update_dpp(0, __builtin_bit_cast(int, v), 0x142, 0xa, 0xf, true));
  v += __builtin_bit_cast(float, __builtin_amdgcn_update_dpp(0, __builtin_bit_cast(int, v), 0x143, 0xc, 0xf, true));
  return __builtin_bit_cast(float, __builtin_amdgcn_readlane(__builtin_bit_cast(int, v), 63));
}
__device__ __forceinline__ float2 cispi_neg(float x) { float s, c; sincospif(x, &s, &c); return make_float2(c, -s); }
__device__ __forceinline__ void pow7(float2 w, float2 (&p)[8]) {
  p[0] = make_float2(1.f, 0.f);
  p[1] = w; p[2] = cmul(w, w); p[3] = cmul(p[2], w); p[4] = cmul(p[2], p[2]);
  p[5] = cmul(p[4], w); p[6] = cmul(p[3], p[3]); p[7] = cmul(p[4], p[3]);
}
#define LGKM0() do { __builtin_amdgcn_s_waitcnt(0xC07F); __builtin_amdgcn_wave_barrier(); } while (0)

template <int SGN> __device__ __forceinline__ void fft512_wave(float2 (&a)[8], float2* bw, int lane) {
  const int l0 = lane & 7, mm = lane >> 3;
  float2 p1[8], p2[8];
  pow7(cispi_neg((float)lane * (1.0f / 256.0f)), p1);
  pow7(cispi_neg((float)l0 * (1.0f / 32.0f)), p2);
  dft8<SGN>(a);
#pragma unroll
  for (int m = 1; m < 8; ++m) a[m] = twm<SGN>(a[m], p1[m]);
#pragma unroll
  for (int m = 0; m < 8; ++m) bw[m * 72 + lane] = a[m];
  LGKM0();
#pragma unroll
  for (int j = 0; j < 8; ++j) a[j] = bw[mm * 72 + l0 + 8 * j];
  LGKM0();
  dft8<SGN>(a);
#pragma unroll
  for (int m = 1; m < 8; ++m) a[m] = twm<SGN>(a[m], p2[m]);
#pragma unroll
  for (int m = 0; m < 8; ++m) bw[l0 * 66 + mm + 8 * m] = a[m];
  LGKM0();
#pragma unroll
  for (int j = 0; j < 8; ++j) a[j] = bw[j * 66 + lane];
  LGKM0();
  dft8<SGN>(a);
}

constexpr int RS_ = 584;

__device__ __forceinline__ int k2_freq(int i, int tau) {
  const int h = tau & 3, m2 = (tau >> 2) & 7, m1 = tau >> 5, e = i >> 2, m4 = i & 3;
  return m1 + 8 * m2 + 64 * (2 * h + e) + 512 * m4;
}
__device__ __forceinline__ float2 rot8(float2 w, int it) {
  const float R = 0.70710678118654752440f;
  return it == 0 ? w : it == 1 ? make_float2((w.x + w.y) * R, (w.y - w.x) * R) : it == 2 ? make_float2(w.y, -w.x) : make_float2((w.y - w.x) * R, -(w.x + w.y) * R);
}

constexpr int ST_AUX = 16;
typedef u32 u32x4s __attribute__((ext_vector_type(4)));
typedef u32 u32x2s __attribute__((ext_vector_type(2)));
__device__ __forceinline__ void gst16(void* base, u32 bytes, int off, u32x4s v) { __builtin_amdgcn_raw_buffer_store_b128(v, __builtin_amdgcn_make_buffer_rsrc(base, 0, bytes, 0x00020000), off, 0, ST_AUX); }
__device__ __forceinline__ void gst8(void* base, u32 bytes, int off, u32x2s v) { __builtin_amdgcn_raw_buffer_store_b64(v, __builtin_amdgcn_make_buffer_rsrc(base, 0, bytes, 0x00020000), off, 0, ST_AUX); }
__global__ __launch_bounds__(512) void k1_ln_rfft(const u16* __restrict__ x, const u16* __restrict__ wr, const u16* __restrict__ wi,
                                                  float2* __restrict__ T, u32* __restrict__ Wt, float4* __restrict__ Cs, u32* __restrict__ flags) {
  __shared__ __attribute__((aligned(16))) float2 buf[8 * RS_];
  const int tid = threadIdx.x, lane = tid & 63, w = tid >> 6;
  const int r = tid & 7;
  if (blockIdx.x >= 256) {
    const int blk = blockIdx.x - 256;
    if (blk < 16) flags[blk * 512 + tid] = 0u;
    u32 (*wsm)[516] = (u32 (*)[516])buf;
    const int f = k2_freq(blk >> 5, ((blk & 31) << 3) + w);
    const u16* pr = wr + (size_t)f * DF_;
    const u16* pi = wi + (size_t)f * DF_;
    u16 wra[8], wia[8];
#pragma unroll
    for (int j = 0; j < 8; ++j) { wra[j] = pr[lane + 64 * j]; wia[j] = pi[lane + 64 * j]; }
    if (lane == 0) {
      const int fm = (2048 - f) & 2047;
      const float w0r = bf2f(pr[0]), w0i = bf2f(pi[0]), w0mr = bf2f(wr[(size_t)fm * DF_]), w0mi = bf2f(wi[(size_t)fm * DF_]);
      const float wnr = bf2f(pr[512]), wni = bf2f(pi[512]), wnmr = bf2f(wr[(size_t)fm * DF_ + 512]), wnmi = bf2f(wi[(size_t)fm * DF_ + 512]);
      const float2 A = make_float2(0.5f * (w0r + w0mr), 0.5f * (w0i - w0mi));
      const float2 B = make_float2(0.5f * (wnr + wnmr), 0.5f * (wni - wnmi));
      Cs[(blk >> 5) * 256 + ((blk & 31) << 3) + w] = make_float4(0.5f * (A.x + B.x), 0.5f * (A.y + B.y), 0.5f * (A.x - B.x), 0.5f * (A.y - B.y));
    }
#pragma unroll
    for (int j = 0; j < 8; ++j) wsm[w][lane + 64 * j] = (u32)wra[j] | ((u32)wia[j] << 16);
    __syncthreads();
    const int i = blk >> 5, tau0 = (blk & 31) << 3;
#pragma unroll
    for (int it = 0; it < 4; ++it) {
      const int kp = (tid >> 3) + 64 * it;
      u32x2 v; v.x = wsm[r][2 * kp]; v.y = wsm[r][2 * kp + 1];
      *(u32x2*)(Wt + ((size_t)kp * 8 + i) * 512 + 2 * (tau0 + r)) = v;
    }
    return;
  }
  const int blk = blockIdx.x;
  const int row = blk * 8 + w;
  const u32* xr = (const u32*)(x + (size_t)row * D_);
  u32 raw[8];
#pragma unroll
  for (int j = 0; j < 8; ++j) raw[j] = xr[lane + 64 * j];
  const float2 wpost = cispi_neg((float)(tid >> 3) * (1.0f / 256.0f));
  float2 a[8];
  float sum = 0.f;
#pragma unroll
  for (int j = 0; j < 8; ++j) { a[j] = make_float2(__uint_as_float(raw[j] << 16), __uint_as_float(raw[j] & 0xffff0000u)); sum += a[j].x + a[j].y; }
  const float mean = wave_sum(sum) * (1.0f / 1024.0f);
  float sq = 0.f;
#pragma unroll
  for (int j = 0; j < 8; ++j) { const float dx = a[j].x - mean, dy = a[j].y - mean; sq += dx * dx + dy * dy; }
  const float var = wave_sum(sq) * (1.0f / 1024.0f);
  const float mb = bfr(mean), rden = 1.0f / bfr(sqrtf(bfr(bfr(var) + 1e-5f)));
#pragma unroll
  for (int j = 0; j < 8; ++j) { a[j].x = bfr(bfr(a[j].x - mb) * rden); a[j].y = bfr(bfr(a[j].y - mb) * rden); }
  float2* bw = buf + w * RS_;
  fft512_wave<-1>(a, bw, lane);
#pragma unroll
  for (int m = 0; m < 8; ++m) bw[lane + 64 * m] = a[m];
  __syncthreads();
  const float2* zr = buf + r * RS_;
  const float2 w1 = make_float2(0.99998117528260111f, -0.0061358846491544753f);
#pragma unroll
  for (int it = 0; it < 4; ++it) {
    const int kp = (tid >> 3) + 64 * it;
    const float2 we = rot8(wpost, it);
    float4 o;
#pragma unroll
    for (int e = 0; e < 2; ++e) {
      const int k = 2 * kp + e;
      const float2 zk = zr[k], zm = zr[(512 - k) & 511];
      const float2 sum2 = make_float2(zk.x + zm.x, zk.y - zm.y);
      const float2 dif2 = make_float2(zk.x - zm.x, zk.y + zm.y);
      const float2 t = cmul(dif2, e ? cmul(we, w1) : we);
      float2 X = make_float2(sum2.x + t.y, sum2.y - t.x);
      if (k == 0) X = make_float2(2.f * (zk.x + zk.y), 2.f * (zk.x - zk.y));
      if (e == 0) { o.x = X.x; o.y = X.y; } else { o.z = X.x; o.w = X.y; }
    }
    gst16(T, 8u << 20, (kp * S_ + blk * 8 + r) * 16, __builtin_bit_cast(u32x4s, o));
  }
}

__device__ __forceinline__ int ex3(int g, int m, int v, int c) { return g * 74 + (m * 4 + v) * 2 + c + (m >> 2) * 8; }
__global__ __launch_bounds__(512) void k2_col(float2* __restrict__ T, const u32* __restrict__ Wt, const float4* __restrict__ Cs,
                                              const u32* __restrict__ pf2) {
  __shared__ __attribute__((aligned(16))) float2 exA[4736];
  __shared__ __attribute__((aligned(16))) float2 exB[4736];
  const int t = threadIdx.x, kp = blockIdx.x, c = t & 1, u = t >> 1;
  float2* col = T + (size_t)kp * S_ * 2;
  float2 a[8];
#pragma unroll
  for (int j = 0; j < 8; ++j) a[j] = col[(u + 256 * j) * 2 + c];
  u32 wv[8];
#pragma unroll
  for (int i = 0; i < 8; ++i) wv[i] = Wt[((size_t)kp * 8 + i) * 512 + t];
  float4 cs[8];
  if (kp == 0) {
#pragma unroll
    for (int i = 0; i < 8; ++i) cs[i] = Cs[i * 256 + u];
  }
  const int u0 = u & 31, m1 = u >> 5;
  const int v0 = u & 3, g = u >> 2;
  float2 p1[8], p2[8], p3[8];
  pow7(cispi_neg((float)u * (1.0f / 1024.0f)), p1);
  pow7(cispi_neg((float)u0 * (1.0f / 128.0f)), p2);
  pow7(cispi_neg((float)v0 * (1.0f / 16.0f)), p3);
  dft8<-1>(a);
#pragma unroll
  for (int m = 1; m < 8; ++m) a[m] = cmul(a[m], p1[m]);
#pragma unroll
  for (int m = 0; m < 8; ++m) exA[(m * 256 + u) * 2 + c] = a[m];
  asm volatile("s_waitcnt vmcnt(0)" ::: "memory");
  __syncthreads();
  const int pfi = (kp * 448 + (t < 448 ? t : 447)) * 16;
  const u32 pfb = pf2[pfi];
#pragma unroll
  for (int j = 0; j < 8; ++j) a[j] = exA[(m1 * 256 + u0 + 32 * j) * 2 + c];
  dft8<-1>(a);
#pragma unroll
  for (int m = 1; m < 8; ++m) a[m] = cmul(a[m], p2[m]);
#pragma unroll
  for (int m = 0; m < 8; ++m) exB[((m1 * 8 + m) * 36 + u0) * 2 + c] = a[m];
  __syncthreads();
#pragma unroll
  for (int j = 0; j < 8; ++j) a[j] = exB[(g * 36 + v0 + 4 * j) * 2 + c];
  dft8<-1>(a);
#pragma unroll
  for (int m = 1; m < 8; ++m) a[m] = cmul(a[m], p3[m]);
#pragma unroll
  for (int m = 0; m < 8; ++m) exA[ex3(g, m, v0, c)] = a[m];
  __syncthreads();
#pragma unroll
  for (int e = 0; e < 2; ++e)
#pragma unroll
    for (int v = 0; v < 4; ++v) a[e * 4 + v] = exA[ex3(g, 2 * v0 + e, v, c)];
  dft4<-1>(a[0], a[1], a[2], a[3]);
  dft4<-1>(a[4], a[5], a[6], a[7]);
  if (kp == 0) {
    if (c == 0) {
#pragma unroll
      for (int i = 0; i < 8; ++i) exB[k2_freq(i, u)] = a[i];
    }
    __syncthreads();
    if (c == 0) {
#pragma unroll
      for (int i = 0; i < 8; ++i) {
        const int f = k2_freq(i, u), fm = (2048 - f) & 2047;
        const float2 pm = exB[fm];
        const float2 C1 = make_float2(cs[i].x, cs[i].y), C2 = make_float2(cs[i].z, cs[i].w);
        a[i] = cadd(cmul(a[i], C1), cmul(make_float2(pm.x, -pm.y), C2));
      }
    } else {
#pragma unroll
      for (int i = 0; i < 8; ++i) a[i] = cmul(a[i], make_float2(__uint_as_float(wv[i] << 16), __uint_as_float(wv[i] & 0xffff0000u)));
    }
    __syncthreads();
  } else {
#pragma unroll
    for (int i = 0; i < 8; ++i) a[i] = cmul(a[i], make_float2(__uint_as_float(wv[i] << 16), __uint_as_float(wv[i] & 0xffff0000u)));
  }
  dft4<1>(a[0], a[1], a[2], a[3]);
  dft4<1>(a[4], a[5], a[6], a[7]);
#pragma unroll
  for (int e = 0; e < 2; ++e)
#pragma unroll
    for (int v = 0; v < 4; ++v) exB[ex3(g, 2 * v0 + e, v, c)] = a[e * 4 + v];
  __syncthreads();
#pragma unroll
  for (int m = 0; m < 8; ++m) a[m] = exB[ex3(g, m, v0, c)];
#pragma unroll
  for (int m = 1; m < 8; ++m) a[m] = cmulc(a[m], p3[m]);
  dft8<1>(a);
#pragma unroll
  for (int j = 0; j < 8; ++j) exA[(g * 36 + v0 + 4 * j) * 2 + c] = a[j];
  __syncthreads();
#pragma unroll
  for (int m = 0; m < 8; ++m) a[m] = exA[((m1 * 8 + m) * 36 + u0) * 2 + c];
#pragma unroll
  for (int m = 1; m < 8; ++m) a[m] = cmulc(a[m], p2[m]);
  dft8<1>(a);
#pragma unroll
  for (int j = 0; j < 8; ++j) exB[(m1 * 256 + u0 + 32 * j) * 2 + c] = a[j];
  __syncthreads();
#pragma unroll
  for (int m = 0; m < 8; ++m) a[m] = exB[(m * 256 + u) * 2 + c];
#pragma unroll
  for (int m = 1; m < 8; ++m) a[m] = cmulc(a[m], p1[m]);
  dft8<1>(a);
#pragma unroll
  for (int j = 0; j < 8; ++j) gst8(T, 8u << 20, ((kp * S_ + u + 256 * j) * 2 + c) * 8, __builtin_bit_cast(u32x2s, a[j]));
  asm volatile("" ::"v"(pfb));
}

__global__ __launch_bounds__(512) void k3_irfft_ln(const float2* __restrict__ T, const u16* __restrict__ x, u16* __restrict__ hout, u16* __restrict__ ln2, const u32* __restrict__ pf1) {
  __shared__ __attribute__((aligned(16))) float2 buf[8 * RS_];
  const int tid = threadIdx.x, lane = tid & 63, w = tid >> 6, blk = blockIdx.x;
  const int row = blk * 8 + w;
  {
    const int r = tid & 7;
    float4 v[4];
#pragma unroll
    for (int it = 0; it < 4; ++it) v[it] = *(const float4*)(T + ((size_t)((tid >> 3) + 64 * it) * S_ + blk * 8 + r) * 2);
#pragma unroll
    for (int it = 0; it < 4; ++it) *(float4*)(buf + r * RS_ + 2 * ((tid >> 3) + 64 * it)) = v[it];
  }
  const u32* xr = (const u32*)(x + (size_t)row * D_);
  u32 raw[8];
#pragma unroll
  for (int j = 0; j < 8; ++j) raw[j] = xr[lane + 64 * j];
  const float2 wl = cispi_neg((float)lane * (1.0f / 512.0f));
  __syncthreads();
  const u32 pfa = pf1[(blk * 448 + (tid < 448 ? tid : 447)) * 16];
  float2* bw = buf + w * RS_;
  float2 a[8];
  const float C8 = 0.92387953251128674f, S8 = 0.38268343236508977f;
  const float2 wl2 = cmul(wl, make_float2(C8, -S8));
#pragma unroll
  for (int j = 0; j < 8; ++j) {
    const int k = lane + 64 * j;
    const float2 yk = bw[k], ym = bw[(512 - k) & 511];
    const float2 sum2 = make_float2(yk.x + ym.x, yk.y - ym.y);
    const float2 dif2 = make_float2(yk.x - ym.x, yk.y + ym.y);
    const float2 t = cmulc(dif2, rot8((j & 1) ? wl2 : wl, j >> 1));
    a[j] = make_float2(sum2.x - t.y, sum2.y + t.x);
    if (k == 0) a[j] = make_float2(yk.x + yk.y, yk.x - yk.y);
  }
  LGKM0();
  fft512_wave<1>(a, bw, lane);
  const float scale = 1.0f / 4194304.0f;
  float sum = 0.f;
#pragma unroll
  for (int j = 0; j < 8; ++j) {
    a[j].x = bfr(__uint_as_float(raw[j] << 16) + bfr(a[j].x * scale));
    a[j].y = bfr(__uint_as_float(raw[j] & 0xffff0000u) + bfr(a[j].y * scale));
    sum += a[j].x + a[j].y;
  }
  u32* wb = (u32*)bw;
#pragma unroll
  for (int j = 0; j < 8; ++j) wb[lane + 64 * j] = pk2(a[j].x, a[j].y);
  const float mean = wave_sum(sum) * (1.0f / 1024.0f);
  float sq = 0.f;
#pragma unroll
  for (int j = 0; j < 8; ++j) { const float dx = a[j].x - mean, dy = a[j].y - mean; sq += dx * dx + dy * dy; }
  const float var = wave_sum(sq) * (1.0f / 1024.0f);
  const float mb = bfr(mean), rden = 1.0f / bfr(sqrtf(bfr(bfr(var) + 1e-5f)));
#pragma unroll
  for (int j = 0; j < 8; ++j) wb[512 + lane + 64 * j] = pk2(bfr(a[j].x - mb) * rden, bfr(a[j].y - mb) * rden);
  LGKM0();
#pragma unroll
  for (int i = 0; i < 2; ++i) {
    gst16(ln2, S_ * D_ * 2, row * 2048 + (lane + 64 * i) * 16, *(const u32x4s*)(wb + 512 + (lane + 64 * i) * 4));
    gst16(hout, S_ * D_ * 2, row * 2048 + (lane + 64 * i) * 16, *(const u32x4s*)(wb + (lane + 64 * i) * 4));
  }
  asm volatile("" ::"v"(pfa));
}

#define WAIT_VM(n) asm volatile("s_waitcnt vmcnt(%0)" ::"n"(n) : "memory")
#define BARRIER() do { asm volatile("s_waitcnt lgkmcnt(0)" ::: "memory"); __builtin_amdgcn_s_barrier(); asm volatile("" ::: "memory"); } while (0)

typedef int i32x4 __attribute__((ext_vector_type(4)));
__device__ __forceinline__ i32x4 mk_rsrc(const void* p, u32 bytes) {
  const unsigned long long a = (unsigned long long)p;
  i32x4 r; r.x = (int)(u32)a; r.y = (int)(u32)(a >> 32); r.z = (int)bytes; r.w = 0x00020000; return r;
}
__device__ __forceinline__ void dma16(i32x4 rs, u32 lds_addr, int voff, int soff) {
  asm volatile("s_mov_b32 m0, %0\n\ts_nop 0\n\tbuffer_load_dwordx4 %1, %2, %3 offen lds" ::"s"(lds_addr), "v"(voff), "s"(rs), "s"(soff) : "memory");
}
__device__ __forceinline__ bf16x8 lds_a(const char* p) { return *(const bf16x8*)p; }
__device__ __forceinline__ bf16x8 lds_tr(const char* p0, const char* p1) {
  s16x4 lo = __builtin_amdgcn_ds_read_tr16_b64_v4i16((LDS_AS s16x4*)p0);
  s16x4 hi = __builtin_amdgcn_ds_read_tr16_b64_v4i16((LDS_AS s16x4*)p1);
  s16x8 v = __builtin_shufflevector(lo, hi, 0, 1, 2, 3, 4, 5, 6, 7);
  return __builtin_bit_cast(bf16x8, v);
}
typedef float f32x2 __attribute__((ext_vector_type(2)));
__device__ __forceinline__ f32x2 mish2(f32x2 h) {
  f32x2 t; t.x = fminf(h.x, 30.f); t.y = fminf(h.y, 30.f);
  t = t * 1.4426950408889634f;
  f32x2 u; u.x = __builtin_amdgcn_exp2f(t.x); u.y = __builtin_amdgcn_exp2f(t.y);
  const f32x2 n = u * (u + 2.f), d = n + 2.f;
  f32x2 r; r.x = __builtin_amdgcn_rcpf(d.x); r.y = __builtin_amdgcn_rcpf(d.y);
  return (h * n) * r;
}

constexpr int G1_STAGE = 16384 + 28672, G1_NST = 3;
__global__ __launch_bounds__(512, 2) void k4_gemm1(const u16* __restrict__ A, const u16* __restrict__ B, const u16* __restrict__ bias, u16* __restrict__ C) {
  extern __shared__ __attribute__((aligned(16))) char sm[];
  const int tid = threadIdx.x, lane = tid & 63, wv = __builtin_amdgcn_readfirstlane(tid >> 6);
  const u32 smbase = (u32)(size_t)(LDS_AS char*)sm;
  const int xcd = blockIdx.x & 7, idx = blockIdx.x >> 3;
  const int tm = (xcd & 1) * 8 + (idx & 7), tn = (xcd >> 1) * 4 + (idx >> 3);
  const int m0 = tm * 128, n0 = tn * 224;
  constexpr int NT = D_ / 64;
  if (wv >= 4) {
    const int w = wv - 4;
    const i32x4 rsA = mk_rsrc(A, S_ * D_ * 2);
    const i32x4 rsB = mk_rsrc(B, D_ * F_ * 2);
    int voA[4], voB[7];
#pragma unroll
    for (int i = 0; i < 4; ++i) { const int row = (w * 4 + i) * 8 + (lane >> 3), cch = (lane & 7) ^ (row & 7); voA[i] = (m0 + row) * (D_ * 2) + cch * 16; }
#pragma unroll
    for (int i = 0; i < 7; ++i) {
      const int L = (w * 7 + i) * 64 + lane, k = L / 28, cc = L % 28, nb = (cc >> 1) ^ ((k >> 3) & 1);
      voB[i] = k * (F_ * 2) + (n0 + nb * 16 + (cc & 1) * 8) * 2;
    }
#define G1_STAGE_LOAD(st, kt) do { const u32 sb_ = smbase + (st) * G1_STAGE; \
    _Pragma("unroll") for (int i = 0; i < 4; ++i) dma16(rsA, sb_ + (w * 4 + i) * 1024, voA[i], (kt) * 128); \
    _Pragma("unroll") for (int i = 0; i < 7; ++i) dma16(rsB, sb_ + 16384 + (w * 7 + i) * 1024, voB[i], (kt) * (64 * F_ * 2)); } while (0)
    G1_STAGE_LOAD(0, 0);
    G1_STAGE_LOAD(1, 1);
    G1_STAGE_LOAD(2, 2);
    WAIT_VM(22);
    BARRIER();
    int st = 0;
    for (int kt = 0; kt < NT; ++kt) {
      if (kt + 2 < NT) WAIT_VM(11); else WAIT_VM(0);
      BARRIER();
      if (kt + 3 < NT) G1_STAGE_LOAD(st, kt + 3);
      st = st == 2 ? 0 : st + 1;
    }
  } else {
    const int wr = wv >> 1, wc = wv & 1, fr = lane & 15, fq = lane >> 4;
    f32x4 acc[4][7];
#pragma unroll
    for (int i = 0; i < 4; ++i)
#pragma unroll
      for (int j = 0; j < 7; ++j) acc[i][j] = (f32x4){0.f, 0.f, 0.f, 0.f};
    const int aoff = (wr * 64 + fr) * 128;
    const int ach0 = ((0 + fq) ^ (fr & 7)) * 16, ach1 = ((4 + fq) ^ (fr & 7)) * 16;
    const int q = (lane & 15) >> 2, p = lane & 3;
    const int boff = 16384 + (8 * fq + q) * 448 + p * 8;
    const int gb = fq & 1;
    bf16x8 ax[4], bx[7], ay[4], by[7];
#define G1_READ(AF, BF, sb, ks) do { \
    _Pragma("unroll") for (int mi = 0; mi < 4; ++mi) AF[mi] = lds_a((sb) + aoff + mi * 2048 + ((ks) ? ach1 : ach0)); \
    _Pragma("unroll") for (int ni = 0; ni < 7; ++ni) { const char* bp = (sb) + boff + (ks) * (32 * 448) + (((wc * 7 + ni) ^ gb) * 32); BF[ni] = lds_tr(bp, bp + 4 * 448); } } while (0)
#define G1_MMA(AF, BF) do { \
    _Pragma("unroll") for (int ni = 0; ni < 7; ++ni) _Pragma("unroll") for (int mi = 0; mi < 4; ++mi) \
      acc[mi][ni] = __builtin_amdgcn_mfma_f32_16x16x32_bf16(BF[ni], AF[mi], acc[mi][ni], 0, 0, 0); } while (0)
#define G1_SCHED() do { _Pragma("unroll") for (int i = 0; i < 18; ++i) { __builtin_amdgcn_sched_group_barrier(0x008, 1, 0); __builtin_amdgcn_sched_group_barrier(0x100, 1, 0); } \
    __builtin_amdgcn_sched_group_barrier(0x008, 10, 0); __builtin_amdgcn_sched_barrier(0); } while (0)
    BARRIER();
    G1_READ(ax, bx, sm, 0);
    int st = 0;
    for (int kt = 0; kt < NT; ++kt) {
      const char* sb = sm + st * G1_STAGE;
      const int sn = st == 2 ? 0 : st + 1;
      __builtin_amdgcn_sched_barrier(0);
      G1_READ(ay, by, sb, 1);
      G1_MMA(ax, bx);
      G1_SCHED();
      BARRIER();
      __builtin_amdgcn_sched_barrier(0);
      G1_READ(ax, bx, sm + sn * G1_STAGE, 0);
      G1_MMA(ay, by);
      G1_SCHED();
      st = sn;
    }
#pragma unroll
    for (int ni = 0; ni < 7; ++ni)
#pragma unroll
      for (int mi = 0; mi < 4; ++mi) *(f32x4*)(sm + (wr * 64 + mi * 16 + fr) * 912 + (wc * 112 + ni * 16 + fq * 4) * 4) = acc[mi][ni];
  }
  BARRIER();
#pragma unroll
  for (int i = 0; i < 7; ++i) {
    const int id = tid + 512 * i, row = id / 28, ch = id % 28;
    const f32x4 v0 = *(const f32x4*)(sm + row * 912 + ch * 32), v1 = *(const f32x4*)(sm + row * 912 + ch * 32 + 16);
    const uint4 bb = *(const uint4*)(bias + n0 + ch * 8);
    const f32x2 r0 = mish2((f32x2){v0[0] + __uint_as_float(bb.x << 16), v0[1] + __uint_as_float(bb.x & 0xffff0000u)});
    const f32x2 r1 = mish2((f32x2){v0[2] + __uint_as_float(bb.y << 16), v0[3] + __uint_as_float(bb.y & 0xffff0000u)});
    const f32x2 r2 = mish2((f32x2){v1[0] + __uint_as_float(bb.z << 16), v1[1] + __uint_as_float(bb.z & 0xffff0000u)});
    const f32x2 r3 = mish2((f32x2){v1[2] + __uint_as_float(bb.w << 16), v1[3] + __uint_as_float(bb.w & 0xffff0000u)});
    uint4 o; o.x = pk2(r0.x, r0.y); o.y = pk2(r1.x, r1.y); o.z = pk2(r2.x, r2.y); o.w = pk2(r3.x, r3.y);
    gst16(C, S_ * LA_ * 2, (m0 + row) * (LA_ * 2) + n0 * 2 + ch * 16, __builtin_bit_cast(u32x4s, o));
  }
}

typedef u32 u32x4 __attribute__((ext_vector_type(4)));
constexpr int G2_STAGE = 32768, G2_NST = 4;
__global__ __launch_bounds__(512, 2) void k5_gemm2(const u16* __restrict__ A, const u16* __restrict__ B, const u16* __restrict__ H, u16* __restrict__ C,
                                                   float* P, u32* flags) {
  extern __shared__ __attribute__((aligned(16))) char sm[];
  const int tid = threadIdx.x, lane = tid & 63, wv = __builtin_amdgcn_readfirstlane(tid >> 6);
  const u32 smbase = (u32)(size_t)(LDS_AS char*)sm;
  const int xcd = blockIdx.x & 7, idx = blockIdx.x >> 3, kh = xcd & 1, pair = (xcd >> 1) * 32 + idx;
  const int tm = (xcd >> 1) * 4 + (idx & 3), tn = idx >> 2;
  const int m0 = tm * 128, n0 = tn * 128, k0 = kh * (F_ / 2);
  constexpr int NT = F_ / 2 / 64;
  const __amdgpu_buffer_rsrc_t rp = __builtin_amdgcn_make_buffer_rsrc((void*)P, 0, 8 << 20, 0x00020000);
  const int fr = lane & 15, fq = lane >> 4;
  if (wv >= 4) {
    const int w = wv - 4;
    const i32x4 rsA = mk_rsrc(A, S_ * LA_ * 2);
    const i32x4 rsB = mk_rsrc(B, F_ * D_ * 2);
    int voA[4], voB[4];
#pragma unroll
    for (int i = 0; i < 4; ++i) { const int row = (w * 4 + i) * 8 + (lane >> 3), cch = (lane & 7) ^ (row & 7); voA[i] = (m0 + row) * (LA_ * 2) + k0 * 2 + cch * 16; }
#pragma unroll
    for (int i = 0; i < 4; ++i) {
      const int k = (w * 4 + i) * 4 + (lane >> 4), cc = lane & 15, f = (k & 3) | (((k >> 3) & 1) << 2), nb = (cc >> 1) ^ f;
      voB[i] = (k0 + k) * (D_ * 2) + (n0 + nb * 16 + (cc & 1) * 8) * 2;
    }
#define G2_STAGE_LOAD(st, kt) do { const u32 sb_ = smbase + (st) * G2_STAGE; \
    _Pragma("unroll") for (int i = 0; i < 4; ++i) dma16(rsA, sb_ + (w * 4 + i) * 1024, voA[i], (kt) * 128); \
    _Pragma("unroll") for (int i = 0; i < 4; ++i) dma16(rsB, sb_ + 16384 + (w * 4 + i) * 1024, voB[i], (kt) * (64 * D_ * 2)); } while (0)
    G2_STAGE_LOAD(0, 0);
    G2_STAGE_LOAD(1, 1);
    G2_STAGE_LOAD(2, 2);
    G2_STAGE_LOAD(3, 3);
    WAIT_VM(24);
    BARRIER();
    int st = 0;
    for (int kt = 0; kt < NT; ++kt) {
      if (kt + 3 < NT) WAIT_VM(16); else if (kt + 2 < NT) WAIT_VM(8); else WAIT_VM(0);
      BARRIER();
      if (kt + 4 < NT) G2_STAGE_LOAD(st, kt + 4);
      st = (st + 1) & 3;
    }
    BARRIER();
  } else {
    const int wr = wv >> 1, wc = wv & 1;
    f32x4 acc[4][4];
#pragma unroll
    for (int i = 0; i < 4; ++i)
#pragma unroll
      for (int j = 0; j < 4; ++j) acc[i][j] = (f32x4){0.f, 0.f, 0.f, 0.f};
    const int aoff = (wr * 64 + fr) * 128;
    const int ach0 = ((0 + fq) ^ (fr & 7)) * 16, ach1 = ((4 + fq) ^ (fr & 7)) * 16;
    const int q = (lane & 15) >> 2, p = lane & 3;
    const int bf_ = q | ((fq & 1) << 2);
    const int boff = 16384 + (8 * fq + q) * 256 + p * 8;
    bf16x8 ax[4], bx[4], ay[4], by[4];
#define G2_READ(AF, BF, sb, ks) do { \
    _Pragma("unroll") for (int mi = 0; mi < 4; ++mi) AF[mi] = lds_a((sb) + aoff + mi * 2048 + ((ks) ? ach1 : ach0)); \
    _Pragma("unroll") for (int ni = 0; ni < 4; ++ni) { const char* bp = (sb) + boff + (ks) * 8192 + (((wc * 4 + ni) ^ bf_) * 32); BF[ni] = lds_tr(bp, bp + 1024); } } while (0)
#define G2_MMA(AF, BF) do { \
    _Pragma("unroll") for (int ni = 0; ni < 4; ++ni) _Pragma("unroll") for (int mi = 0; mi < 4; ++mi) \
      acc[mi][ni] = __builtin_amdgcn_mfma_f32_16x16x32_bf16(BF[ni], AF[mi], acc[mi][ni], 0, 0, 0); } while (0)
#define G2_SCHED() do { _Pragma("unroll") for (int i = 0; i < 12; ++i) { __builtin_amdgcn_sched_group_barrier(0x008, 1, 0); __builtin_amdgcn_sched_group_barrier(0x100, 1, 0); } \
    __builtin_amdgcn_sched_group_barrier(0x008, 4, 0); __builtin_amdgcn_sched_barrier(0); } while (0)
    BARRIER();
    G2_READ(ax, bx, sm, 0);
    int st = 0;
    for (int kt = 0; kt < NT; ++kt) {
      const char* sb = sm + st * G2_STAGE;
      const int sn = (st + 1) & 3;
      __builtin_amdgcn_sched_barrier(0);
      G2_READ(ay, by, sb, 1);
      G2_MMA(ax, bx);
      G2_SCHED();
      BARRIER();
      __builtin_amdgcn_sched_barrier(0);
      G2_READ(ax, bx, sm + sn * G2_STAGE, 0);
      G2_MMA(ay, by);
      G2_SCHED();
      st = sn;
    }
    if (wr != kh) {
      const int base = ((pair * 2 + kh) * 2 + wc) * 16384 + lane * 16;
#pragma unroll
      for (int mi = 0; mi < 4; ++mi)
#pragma unroll
        for (int ni = 0; ni < 4; ++ni) __builtin_amdgcn_raw_buffer_store_b128(__builtin_bit_cast(u32x4, acc[mi][ni]), rp, base + (mi * 4 + ni) * 1024, 0, 16);
      WAIT_VM(0);
      if (lane == 0) __hip_atomic_store(flags + ((pair * 2 + kh) * 2 + wc) * 16, 1u, __ATOMIC_RELAXED, __HIP_MEMORY_SCOPE_AGENT);
      BARRIER();
    } else {
      u32* fl = flags + ((pair * 2 + (kh ^ 1)) * 2 + wc) * 16;
      for (int spin = 0; spin < (1 << 20); ++spin) {
        if (__hip_atomic_load(fl, __ATOMIC_RELAXED, __HIP_MEMORY_SCOPE_AGENT) != 0u) break;
        __builtin_amdgcn_s_sleep(1);
      }
      BARRIER();
      const int base = ((pair * 2 + (kh ^ 1)) * 2 + wc) * 16384 + lane * 16;
      u32x4 pv[16];
#pragma unroll
      for (int i = 0; i < 16; ++i) pv[i] = __builtin_amdgcn_raw_buffer_load_b128(rp, base + i * 1024, 0, 16);
#pragma unroll
      for (int mi = 0; mi < 4; ++mi)
#pragma unroll
        for (int ni = 0; ni < 4; ++ni)
          *(f32x4*)(sm + (mi * 16 + fr) * 528 + (wc * 64 + ni * 16 + fq * 4) * 4) = acc[mi][ni] + __builtin_bit_cast(f32x4, pv[mi * 4 + ni]);
    }
  }
  const int mo = m0 + kh * 64;
  uint4 hv[2];
#pragma unroll
  for (int i = 0; i < 2; ++i) { const int id = tid + 512 * i, row = id >> 4, ch = id & 15; hv[i] = *(const uint4*)(H + (size_t)(mo + row) * D_ + n0 + ch * 8); }
  BARRIER();
#pragma unroll
  for (int i = 0; i < 2; ++i) {
    const int id = tid + 512 * i, row = id >> 4, ch = id & 15;
    const f32x4 s0 = *(const f32x4*)(sm + row * 528 + ch * 32), s1 = *(const f32x4*)(sm + row * 528 + ch * 32 + 16);
    uint4 o;
    o.x = pk2(__uint_as_float(hv[i].x << 16) + bfr(s0[0]), __uint_as_float(hv[i].x & 0xffff0000u) + bfr(s0[1]));
    o.y = pk2(__uint_as_float(hv[i].y << 16) + bfr(s0[2]), __uint_as_float(hv[i].y & 0xffff0000u) + bfr(s0[3]));
    o.z = pk2(__uint_as_float(hv[i].z << 16) + bfr(s1[0]), __uint_as_float(hv[i].z & 0xffff0000u) + bfr(s1[1]));
    o.w = pk2(__uint_as_float(hv[i].w << 16) + bfr(s1[2]), __uint_as_float(hv[i].w & 0xffff0000u) + bfr(s1[3]));
    *(uint4*)(C + (size_t)(mo + row) * D_ + n0 + ch * 8) = o;
  }
}

extern "C" void kernel_launch(void* const* d_in, const int* in_sizes, int n_in, void* d_out, int out_size, void* d_ws, size_t ws_size,
                              hipStream_t stream) {
  const u16* x = (const u16*)d_in[0];
  const u16* wr = (const u16*)d_in[1];
  const u16* wi = (const u16*)d_in[2];
  const u16* w1 = (const u16*)d_in[4];
  const u16* b1 = (const u16*)d_in[5];
  const u16* w2 = (const u16*)d_in[6];
  char* ws = (char*)d_ws;
  float2* T = (float2*)ws;
  u32* Wt = (u32*)(ws + (8u << 20));
  u16* act = (u16*)ws;
  u16* h = (u16*)(ws + (16u << 20));
  u16* ln2 = (u16*)(ws + (20u << 20));
  float* P = (float*)(ws + (20u << 20));
  float4* Cs = (float4*)(ws + (28u << 20));
  u32* flags = (u32*)(ws + (28u << 20) + (64u << 10));
  (void)hipFuncSetAttribute((const void*)k4_gemm1, hipFuncAttributeMaxDynamicSharedMemorySize, G1_STAGE * G1_NST);
  (void)hipFuncSetAttribute((const void*)k5_gemm2, hipFuncAttributeMaxDynamicSharedMemorySize, G2_STAGE * G2_NST);
  k1_ln_rfft<<<512, 512, 0, stream>>>(x, wr, wi, T, Wt, Cs, flags);
  k2_col<<<256, 512, 0, stream>>>(T, Wt, Cs, (const u32*)w2);
  k3_irfft_ln<<<256, 512, 0, stream>>>(T, x, h, ln2, (const u32*)w1);
  k4_gemm1<<<256, 512, G1_STAGE * G1_NST, stream>>>(ln2, w1, b1, act);
  k5_gemm2<<<256, 512, G2_STAGE * G2_NST, stream>>>(act, w2, h, (u16*)d_out, P, flags);
}
```

```cpp
#include <hip/hip_runtime.h>
#include <hip/hip_bf16.h>

typedef unsigned short u16;
typedef unsigned int u32;
typedef short s16x4 __attribute__((ext_vector_type(4)));
typedef short s16x8 __attribute__((ext_vector_type(8)));
typedef __bf16 bf16x8 __attribute__((ext_vector_type(8)));
typedef float f32x4 __attribute__((ext_vector_type(4)));
typedef u32 u32x2 __attribute__((ext_vector_type(2)));
#define LDS_AS __attribute__((address_space(3)))

constexpr int S_ = 2048, D_ = 1024, F_ = 3584, DF_ = 513;
constexpr int LA_ = 3648;

__device__ __forceinline__ float bf2f(u16 v) { return __uint_as_float(((u32)v) << 16); }
__device__ __forceinline__ u16 f2bf(float f) { return __builtin_bit_cast(u16, (__bf16)f); }
__device__ __forceinline__ float bfr(float f) { return bf2f(f2bf(f)); }
__device__ __forceinline__ u32 pk2(float lo, float hi) { return (u32)f2bf(lo) | ((u32)f2bf(hi) << 16); }

__device__ __forceinline__ float2 cadd(float2 a, float2 b) { return make_float2(a.x + b.x, a.y + b.y); }
__device__ __forceinline__ float2 csub(float2 a, float2 b) { return make_float2(a.x - b.x, a.y - b.y); }
__device__ __forceinline__ float2 cmul(float2 a, float2 b) { return make_float2(a.x * b.x - a.y * b.y, a.x * b.y + a.y * b.x); }
__device__ __forceinline__ float2 cmulc(float2 a, float2 b) { return make_float2(a.x * b.x + a.y * b.y, a.y * b.x - a.x * b.y); }
template <int SGN> __device__ __forceinline__ float2 twm(float2 a, float2 w) { return SGN < 0 ? cmul(a, w) : cmulc(a, w); }
template <int SGN> __device__ __forceinline__ float2 muli(float2 a) { return SGN < 0 ? make_float2(a.y, -a.x) : make_float2(-a.y, a.x); }

template <int SGN> __device__ __forceinline__ void dft4(float2& b0, float2& b1, float2& b2, float2& b3) {
  float2 s02 = cadd(b0, b2), d02 = csub(b0, b2), s13 = cadd(b1, b3), d13 = muli<SGN>(csub(b1, b3));
  b0 = cadd(s02, s13); b2 = csub(s02, s13); b1 = cadd(d02, d13); b3 = csub(d02, d13);
}
template <int SGN> __device__ __forceinline__ void dft8(float2 (&a)[8]) {
  const float R = 0.70710678118654752440f;
  float2 e0 = cadd(a[0], a[4]), e1 = cadd(a[1], a[5]), e2 = cadd(a[2], a[6]), e3 = cadd(a[3], a[7]);
  float2 o0 = csub(a[0], a[4]), o1 = csub(a[1], a[5]), o2 = csub(a[2], a[6]), o3 = csub(a[3], a[7]);
  if (SGN < 0) {
    o1 = make_float2((o1.x + o1.y) * R, (o1.y - o1.x) * R);
    o3 = make_float2((o3.y - o3.x) * R, -(o3.x + o3.y) * R);
  } else {
    o1 = make_float2((o1.x - o1.y) * R, (o1.x + o1.y) * R);
    o3 = make_float2(-(o3.x + o3.y) * R, (o3.x - o3.y) * R);
  }
  o2 = muli<SGN>(o2);
  dft4<SGN>(e0, e1, e2, e3);
  dft4<SGN>(o0, o1, o2, o3);
  a[0] = e0; a[2] = e1; a[4] = e2; a[6] = e3;
  a[1] = o0; a[3] = o1; a[5] = o2; a[7] = o3;
}

__device__ __forceinline__ float wave_sum(float v) {
  v += __builtin_bit_cast(float, __builtin_amdgcn_update_dpp(0, __builtin_bit_cast(int, v), 0x111, 0xf, 0xf, true));
  v += __builtin_bit_cast(float, __builtin_amdgcn_update_dpp(0, __builtin_bit_cast(int, v), 0x112, 0xf, 0xf, true));
  v += __builtin_bit_cast(float, __builtin_amdgcn_update_dpp(0, __builtin_bit_cast(int, v), 0x114, 0xf, 0xf, true));
  v += __builtin_bit_cast(float, __builtin_amdgcn_update_dpp(0, __builtin_bit_cast(int, v), 0x118, 0xf, 0xf, true));
  v += __builtin_bit_cast(float, __builtin_amdgcn_update_dpp(0, __builtin_bit_cast(int, v), 0x142, 0xa, 0xf, true));
  v += __builtin_bit_cast(float, __builtin_amdgcn_update_dpp(0, __builtin_bit_cast(int, v), 0x143, 0xc, 0xf, true));
  return __builtin_bit_cast(float, __builtin_amdgcn_readlane(__builtin_bit_cast(int, v), 63));
}
__device__ __forceinline__ float2 cispi_neg(float x) { float s, c; sincospif(x, &s, &c); return make_float2(c, -s); }
__device__ __forceinline__ void pow7(float2 w, float2 (&p)[8]) {
  p[0] = make_float2(1.f, 0.f);
  p[1] = w; p[2] = cmul(w, w); p[3] = cmul(p[2], w); p[4] = cmul(p[2], p[2]);
  p[5] = cmul(p[4], w); p[6] = cmul(p[3], p[3]); p[7] = cmul(p[4], p[3]);
}
#define LGKM0() do { __builtin_amdgcn_s_waitcnt(0xC07F); __builtin_amdgcn_wave_barrier(); } while (0)

template <int SGN> __device__ __forceinline__ void fft512_wave(float2 (&a)[8], float2* bw, int lane) {
  const int l0 = lane & 7, mm = lane >> 3;
  float2 p1[8], p2[8];
  pow7(cispi_neg((float)lane * (1.0f / 256.0f)), p1);
  pow7(cispi_neg((float)l0 * (1.0f / 32.0f)), p2);
  dft8<SGN>(a);
#pragma unroll
  for (int m = 1; m < 8; ++m) a[m] = twm<SGN>(a[m], p1[m]);
#pragma unroll
  for (int m = 0; m < 8; ++m) bw[m * 72 + lane] = a[m];
  LGKM0();
#pragma unroll
  for (int j = 0; j < 8; ++j) a[j] = bw[mm * 72 + l0 + 8 * j];
  LGKM0();
  dft8<SGN>(a);
#pragma unroll
  for (int m = 1; m < 8; ++m) a[m] = twm<SGN>(a[m], p2[m]);
#pragma unroll
  for (int m = 0; m < 8; ++m) bw[l0 * 66 + mm + 8 * m] = a[m];
  LGKM0();
#pragma unroll
  for (int j = 0; j < 8; ++j) a[j] = bw[j * 66 + lane];
  LGKM0();
  dft8<SGN>(a);
}

constexpr int RS_ = 584;

__device__ __forceinline__ int k2_freq(int i, int tau) {
  const int h = tau & 3, m2 = (tau >> 2) & 7, m1 = tau >> 5, e = i >> 2, m4 = i & 3;
  return m1 + 8 * m2 + 64 * (2 * h + e) + 512 * m4;
}
__device__ __forceinline__ float2 rot8(float2 w, int it) {
  const float R = 0.70710678118654752440f;
  return it == 0 ? w : it == 1 ? make_float2((w.x + w.y) * R, (w.y - w.x) * R) : it == 2 ? make_float2(w.y, -w.x) : make_float2((w.y - w.x) * R, -(w.x + w.y) * R);
}

constexpr int ST_AUX = 16;
typedef u32 u32x4s __attribute__((ext_vector_type(4)));
typedef u32 u32x2s __attribute__((ext_vector_type(2)));
__device__ __forceinline__ void gst16(void* base, u32 bytes, int off, u32x4s v) { __builtin_amdgcn_raw_buffer_store_b128(v, __builtin_amdgcn_make_buffer_rsrc(base, 0, bytes, 0x00020000), off, 0, ST_AUX); }
__device__ __forceinline__ void gst8(void* base, u32 bytes, int off, u32x2s v) { __builtin_amdgcn_raw_buffer_store_b64(v, __builtin_amdgcn_make_buffer_rsrc(base, 0, bytes, 0x00020000), off, 0, ST_AUX); }
__global__ __launch_bounds__(512) void k1_ln_rfft(const u16* __restrict__ x, const u16* __restrict__ wr, const u16* __restrict__ wi,
                                                  float2* __restrict__ T, u32* __restrict__ Wt, float4* __restrict__ Cs, u32* __restrict__ flags) {
  __shared__ __attribute__((aligned(16))) float2 buf[8 * RS_];
  const int tid = threadIdx.x, lane = tid & 63, w = tid >> 6;
  const int r = tid & 7;
  if (blockIdx.x >= 256) {
    const int blk = blockIdx.x - 256;
    if (blk < 16) flags[blk * 512 + tid] = 0u;
    u32 cpf = 0;
    if (blk >= 16 && blk < 24) {
      u32 lo[4], hi[4];
      asm volatile("s_getpc_b64 s[20:21]\n\ts_add_u32 %0, s20, k2_col@rel32@lo+4\n\ts_addc_u32 %1, s21, k2_col@rel32@hi+12" : "=s"(lo[0]), "=s"(hi[0]) : : "s20", "s21", "scc");
      asm volatile("s_getpc_b64 s[20:21]\n\ts_add_u32 %0, s20, k3_irfft_ln@rel32@lo+4\n\ts_addc_u32 %1, s21, k3_irfft_ln@rel32@hi+12" : "=s"(lo[1]), "=s"(hi[1]) : : "s20", "s21", "scc");
      asm volatile("s_getpc_b64 s[20:21]\n\ts_add_u32 %0, s20, k4_gemm1@rel32@lo+4\n\ts_addc_u32 %1, s21, k4_gemm1@rel32@hi+12" : "=s"(lo[2]), "=s"(hi[2]) : : "s20", "s21", "scc");
      asm volatile("s_getpc_b64 s[20:21]\n\ts_add_u32 %0, s20, k5_gemm2@rel32@lo+4\n\ts_addc_u32 %1, s21, k5_gemm2@rel32@hi+12" : "=s"(lo[3]), "=s"(hi[3]) : : "s20", "s21", "scc");
      const int fn = tid >> 7, li = tid & 127, lim = fn == 0 ? 128 : fn == 1 ? 96 : fn == 2 ? 128 : 64;
      const u32 l_ = fn == 0 ? lo[0] : fn == 1 ? lo[1] : fn == 2 ? lo[2] : lo[3], h_ = fn == 0 ? hi[0] : fn == 1 ? hi[1] : fn == 2 ? hi[2] : hi[3];
      const u32* cp = (const u32*)(((unsigned long long)h_ << 32) | l_);
      cpf = cp[(li < lim ? li : 0) * 16];
    }
    u32 (*wsm)[516] = (u32 (*)[516])buf;
    const int f = k2_freq(blk >> 5, ((blk & 31) << 3) + w);
    const u16* pr = wr + (size_t)f * DF_;
    const u16* pi = wi + (size_t)f * DF_;
    u16 wra[8], wia[8];
#pragma unroll
    for (int j = 0; j < 8; ++j) { wra[j] = pr[lane + 64 * j]; wia[j] = pi[lane + 64 * j]; }
    if (lane == 0) {
      const int fm = (2048 - f) & 2047;
      const float w0r = bf2f(pr[0]), w0i = bf2f(pi[0]), w0mr = bf2f(wr[(size_t)fm * DF_]), w0mi = bf2f(wi[(size_t)fm * DF_]);
      const float wnr = bf2f(pr[512]), wni = bf2f(pi[512]), wnmr = bf2f(wr[(size_t)fm * DF_ + 512]), wnmi = bf2f(wi[(size_t)fm * DF_ + 512]);
      const float2 A = make_float2(0.5f * (w0r + w0mr), 0.5f * (w0i - w0mi));
      const float2 B = make_float2(0.5f * (wnr + wnmr), 0.5f * (wni - wnmi));
      Cs[(blk >> 5) * 256 + ((blk & 31) << 3) + w] = make_float4(0.5f * (A.x + B.x), 0.5f * (A.y + B.y), 0.5f * (A.x - B.x), 0.5f * (A.y - B.y));
    }
#pragma unroll
    for (int j = 0; j < 8; ++j) wsm[w][lane + 64 * j] = (u32)wra[j] | ((u32)wia[j] << 16);
    __syncthreads();
    const int i = blk >> 5, tau0 = (blk & 31) << 3;
#pragma unroll
    for (int it = 0; it < 4; ++it) {
      const int kp = (tid >> 3) + 64 * it;
      u32x2 v; v.x = wsm[r][2 * kp]; v.y = wsm[r][2 * kp + 1];
      *(u32x2*)(Wt + ((size_t)kp * 8 + i) * 512 + 2 * (tau0 + r)) = v;
    }
    asm volatile("" ::"v"(cpf));
    return;
  }
  const int blk = blockIdx.x;
  const int row = blk * 8 + w;
  const u32* xr = (const u32*)(x + (size_t)row * D_);
  u32 raw[8];
#pragma unroll
  for (int j = 0; j < 8; ++j) raw[j] = xr[lane + 64 * j];
  const float2 wpost = cispi_neg((float)(tid >> 3) * (1.0f / 256.0f));
  float2 a[8];
  float sum = 0.f;
#pragma unroll
  for (int j = 0; j < 8; ++j) { a[j] = make_float2(__uint_as_float(raw[j] << 16), __uint_as_float(raw[j] & 0xffff0000u)); sum += a[j].x + a[j].y; }
  const float mean = wave_sum(sum) * (1.0f / 1024.0f);
  float sq = 0.f;
#pragma unroll
  for (int j = 0; j < 8; ++j) { const float dx = a[j].x - mean, dy = a[j].y - mean; sq += dx * dx + dy * dy; }
  const float var = wave_sum(sq) * (1.0f / 1024.0f);
  const float mb = bfr(mean), rden = 1.0f / bfr(sqrtf(bfr(bfr(var) + 1e-5f)));
#pragma unroll
  for (int j = 0; j < 8; ++j) { a[j].x = bfr(bfr(a[j].x - mb) * rden); a[j].y = bfr(bfr(a[j].y - mb) * rden); }
  float2* bw = buf + w * RS_;
  fft512_wave<-1>(a, bw, lane);
#pragma unroll
  for (int m = 0; m < 8; ++m) bw[lane + 64 * m] = a[m];
  __syncthreads();
  const float2* zr = buf + r * RS_;
  const float2 w1 = make_float2(0.99998117528260111f, -0.0061358846491544753f);
#pragma unroll
  for (int it = 0; it < 4; ++it) {
    const int kp = (tid >> 3) + 64 * it;
    const float2 we = rot8(wpost, it);
    float4 o;
#pragma unroll
    for (int e = 0; e < 2; ++e) {
      const int k = 2 * kp + e;
      const float2 zk = zr[k], zm = zr[(512 - k) & 511];
      const float2 sum2 = make_float2(zk.x + zm.x, zk.y - zm.y);
      const float2 dif2 = make_float2(zk.x - zm.x, zk.y + zm.y);
      const float2 t = cmul(dif2, e ? cmul(we, w1) : we);
      float2 X = make_float2(sum2.x + t.y, sum2.y - t.x);
      if (k == 0) X = make_float2(2.f * (zk.x + zk.y), 2.f * (zk.x - zk.y));
      if (e == 0) { o.x = X.x; o.y = X.y; } else { o.z = X.x; o.w = X.y; }
    }
    gst16(T, 8u << 20, (kp * S_ + blk * 8 + r) * 16, __builtin_bit_cast(u32x4s, o));
  }
}

__device__ __forceinline__ int ex3(int g, int m, int v, int c) { return g * 74 + (m * 4 + v) * 2 + c + (m >> 2) * 8; }
extern "C" __global__ __launch_bounds__(512) void k2_col(float2* __restrict__ T, const u32* __restrict__ Wt, const float4* __restrict__ Cs,
                                              const u32* __restrict__ pf2) {
  __shared__ __attribute__((aligned(16))) float2 exA[4736];
  __shared__ __attribute__((aligned(16))) float2 exB[4736];
  const int t = threadIdx.x, kp = blockIdx.x, c = t & 1, u = t >> 1;
  float2* col = T + (size_t)kp * S_ * 2;
  float2 a[8];
#pragma unroll
  for (int j = 0; j < 8; ++j) a[j] = col[(u + 256 * j) * 2 + c];
  u32 wv[8];
#pragma unroll
  for (int i = 0; i < 8; ++i) wv[i] = Wt[((size_t)kp * 8 + i) * 512 + t];
  float4 cs[8];
  if (kp == 0) {
#pragma unroll
    for (int i = 0; i < 8; ++i) cs[i] = Cs[i * 256 + u];
  }
  const int u0 = u & 31, m1 = u >> 5;
  const int v0 = u & 3, g = u >> 2;
  float2 p1[8], p2[8], p3[8];
  pow7(cispi_neg((float)u * (1.0f / 1024.0f)), p1);
  pow7(cispi_neg((float)u0 * (1.0f / 128.0f)), p2);
  pow7(cispi_neg((float)v0 * (1.0f / 16.0f)), p3);
  dft8<-1>(a);
#pragma unroll
  for (int m = 1; m < 8; ++m) a[m] = cmul(a[m], p1[m]);
#pragma unroll
  for (int m = 0; m < 8; ++m) exA[(m * 256 + u) * 2 + c] = a[m];
  asm volatile("s_waitcnt vmcnt(0)" ::: "memory");
  __syncthreads();
  const int pfi = (kp * 448 + (t < 448 ? t : 447)) * 16;
  const u32 pfb = pf2[pfi];
#pragma unroll
  for (int j = 0; j < 8; ++j) a[j] = exA[(m1 * 256 + u0 + 32 * j) * 2 + c];
  dft8<-1>(a);
#pragma unroll
  for (int m = 1; m < 8; ++m) a[m] = cmul(a[m], p2[m]);
#pragma unroll
  for (int m = 0; m < 8; ++m) exB[((m1 * 8 + m) * 36 + u0) * 2 + c] = a[m];
  __syncthreads();
#pragma unroll
  for (int j = 0; j < 8; ++j) a[j] = exB[(g * 36 + v0 + 4 * j) * 2 + c];
  dft8<-1>(a);
#pragma unroll
  for (int m = 1; m < 8; ++m) a[m] = cmul(a[m], p3[m]);
#pragma unroll
  for (int m = 0; m < 8; ++m) exA[ex3(g, m, v0, c)] = a[m];
  __syncthreads();
#pragma unroll
  for (int e = 0; e < 2; ++e)
#pragma unroll
    for (int v = 0; v < 4; ++v) a[e * 4 + v] = exA[ex3(g, 2 * v0 + e, v, c)];
  dft4<-1>(a[0], a[1], a[2], a[3]);
  dft4<-1>(a[4], a[5], a[6], a[7]);
  if (kp == 0) {
    if (c == 0) {
#pragma unroll
      for (int i = 0; i < 8; ++i) exB[k2_freq(i, u)] = a[i];
    }
    __syncthreads();
    if (c == 0) {
#pragma unroll
      for (int i = 0; i < 8; ++i) {
        const int f = k2_freq(i, u), fm = (2048 - f) & 2047;
        const float2 pm = exB[fm];
        const float2 C1 = make_float2(cs[i].x, cs[i].y), C2 = make_float2(cs[i].z, cs[i].w);
        a[i] = cadd(cmul(a[i], C1), cmul(make_float2(pm.x, -pm.y), C2));
      }
    } else {
#pragma unroll
      for (int i = 0; i < 8; ++i) a[i] = cmul(a[i], make_float2(__uint_as_float(wv[i] << 16), __uint_as_float(wv[i] & 0xffff0000u)));
    }
    __syncthreads();
  } else {
#pragma unroll
    for (int i = 0; i < 8; ++i) a[i] = cmul(a[i], make_float2(__uint_as_float(wv[i] << 16), __uint_as_float(wv[i] & 0xffff0000u)));
  }
  dft4<1>(a[0], a[1], a[2], a[3]);
  dft4<1>(a[4], a[5], a[6], a[7]);
#pragma unroll
  for (int e = 0; e < 2; ++e)
#pragma unroll
    for (int v = 0; v < 4; ++v) exB[ex3(g, 2 * v0 + e, v, c)] = a[e * 4 + v];
  __syncthreads();
#pragma unroll
  for (int m = 0; m < 8; ++m) a[m] = exB[ex3(g, m, v0, c)];
#pragma unroll
  for (int m = 1; m < 8; ++m) a[m] = cmulc(a[m], p3[m]);
  dft8<1>(a);
#pragma unroll
  for (int j = 0; j < 8; ++j) exA[(g * 36 + v0 + 4 * j) * 2 + c] = a[j];
  __syncthreads();
#pragma unroll
  for (int m = 0; m < 8; ++m) a[m] = exA[((m1 * 8 + m) * 36 + u0) * 2 + c];
#pragma unroll
  for (int m = 1; m < 8; ++m) a[m] = cmulc(a[m], p2[m]);
  dft8<1>(a);
#pragma unroll
  for (int j = 0; j < 8; ++j) exB[(m1 * 256 + u0 + 32 * j) * 2 + c] = a[j];
  __syncthreads();
#pragma unroll
  for (int m = 0; m < 8; ++m) a[m] = exB[(m * 256 + u) * 2 + c];
#pragma unroll
  for (int m = 1; m < 8; ++m) a[m] = cmulc(a[m], p1[m]);
  dft8<1>(a);
#pragma unroll
  for (int j = 0; j < 8; ++j) gst8(T, 8u << 20, ((kp * S_ + u + 256 * j) * 2 + c) * 8, __builtin_bit_cast(u32x2s, a[j]));
  asm volatile("" ::"v"(pfb));
}

extern "C" __global__ __launch_bounds__(512) void k3_irfft_ln(const float2* __restrict__ T, const u16* __restrict__ x, u16* __restrict__ hout, u16* __restrict__ ln2, const u32* __restrict__ pf1) {
  __shared__ __attribute__((aligned(16))) float2 buf[8 * RS_];
  const int tid = threadIdx.x, lane = tid & 63, w = tid >> 6, blk = blockIdx.x;
  const int row = blk * 8 + w;
  {
    const int r = tid & 7;
    float4 v[4];
#pragma unroll
    for (int it = 0; it < 4; ++it) v[it] = *(const float4*)(T + ((size_t)((tid >> 3) + 64 * it) * S_ + blk * 8 + r) * 2);
#pragma unroll
    for (int it = 0; it < 4; ++it) *(float4*)(buf + r * RS_ + 2 * ((tid >> 3) + 64 * it)) = v[it];
  }
  const u32* xr = (const u32*)(x + (size_t)row * D_);
  u32 raw[8];
#pragma unroll
  for (int j = 0; j < 8; ++j) raw[j] = xr[lane + 64 * j];
  const float2 wl = cispi_neg((float)lane * (1.0f / 512.0f));
  __syncthreads();
  const u32 pfa = pf1[(blk * 448 + (tid < 448 ? tid : 447)) * 16];
  float2* bw = buf + w * RS_;
  float2 a[8];
  const float C8 = 0.92387953251128674f, S8 = 0.38268343236508977f;
  const float2 wl2 = cmul(wl, make_float2(C8, -S8));
#pragma unroll
  for (int j = 0; j < 8; ++j) {
    const int k = lane + 64 * j;
    const float2 yk = bw[k], ym = bw[(512 - k) & 511];
    const float2 sum2 = make_float2(yk.x + ym.x, yk.y - ym.y);
    const float2 dif2 = make_float2(yk.x - ym.x, yk.y + ym.y);
    const float2 t = cmulc(dif2, rot8((j & 1) ? wl2 : wl, j >> 1));
    a[j] = make_float2(sum2.x - t.y, sum2.y + t.x);
    if (k == 0) a[j] = make_float2(yk.x + yk.y, yk.x - yk.y);
  }
  LGKM0();
  fft512_wave<1>(a, bw, lane);
  const float scale = 1.0f / 4194304.0f;
  float sum = 0.f;
#pragma unroll
  for (int j = 0; j < 8; ++j) {
    a[j].x = bfr(__uint_as_float(raw[j] << 16) + bfr(a[j].x * scale));
    a[j].y = bfr(__uint_as_float(raw[j] & 0xffff0000u) + bfr(a[j].y * scale));
    sum += a[j].x + a[j].y;
  }
  u32* wb = (u32*)bw;
#pragma unroll
  for (int j = 0; j < 8; ++j) wb[lane + 64 * j] = pk2(a[j].x, a[j].y);
  const float mean = wave_sum(sum) * (1.0f / 1024.0f);
  float sq = 0.f;
#pragma unroll
  for (int j = 0; j < 8; ++j) { const float dx = a[j].x - mean, dy = a[j].y - mean; sq += dx * dx + dy * dy; }
  const float var = wave_sum(sq) * (1.0f / 1024.0f);
  const float mb = bfr(mean), rden = 1.0f / bfr(sqrtf(bfr(bfr(var) + 1e-5f)));
#pragma unroll
  for (int j = 0; j < 8; ++j) wb[512 + lane + 64 * j] = pk2(bfr(a[j].x - mb) * rden, bfr(a[j].y - mb) * rden);
  LGKM0();
#pragma unroll
  for (int i = 0; i < 2; ++i) {
    gst16(ln2, S_ * D_ * 2, row * 2048 + (lane + 64 * i) * 16, *(const u32x4s*)(wb + 512 + (lane + 64 * i) * 4));
    gst16(hout, S_ * D_ * 2, row * 2048 + (lane + 64 * i) * 16, *(const u32x4s*)(wb + (lane + 64 * i) * 4));
  }
  asm volatile("" ::"v"(pfa));
}

#define WAIT_VM(n) asm volatile("s_waitcnt vmcnt(%0)" ::"n"(n) : "memory")
#define BARRIER() do { asm volatile("s_waitcnt lgkmcnt(0)" ::: "memory"); __builtin_amdgcn_s_barrier(); asm volatile("" ::: "memory"); } while (0)

typedef int i32x4 __attribute__((ext_vector_type(4)));
__device__ __forceinline__ i32x4 mk_rsrc(const void* p, u32 bytes) {
  const unsigned long long a = (unsigned long long)p;
  i32x4 r; r.x = (int)(u32)a; r.y = (int)(u32)(a >> 32); r.z = (int)bytes; r.w = 0x00020000; return r;
}
__device__ __forceinline__ void dma16(i32x4 rs, u32 lds_addr, int voff, int soff) {
  asm volatile("s_mov_b32 m0, %0\n\ts_nop 0\n\tbuffer_load_dwordx4 %1, %2, %3 offen lds" ::"s"(lds_addr), "v"(voff), "s"(rs), "s"(soff) : "memory");
}
__device__ __forceinline__ bf16x8 lds_a(const char* p) { return *(const bf16x8*)p; }
__device__ __forceinline__ bf16x8 lds_tr(const char* p0, const char* p1) {
  s16x4 lo = __builtin_amdgcn_ds_read_tr16_b64_v4i16((LDS_AS s16x4*)p0);
  s16x4 hi = __builtin_amdgcn_ds_read_tr16_b64_v4i16((LDS_AS s16x4*)p1);
  s16x8 v = __builtin_shufflevector(lo, hi, 0, 1, 2, 3, 4, 5, 6, 7);
  return __builtin_bit_cast(bf16x8, v);
}
typedef float f32x2 __attribute__((ext_vector_type(2)));
__device__ __forceinline__ f32x2 mish2(f32x2 h) {
  f32x2 t; t.x = fminf(h.x, 30.f); t.y = fminf(h.y, 30.f);
  t = t * 1.4426950408889634f;
  f32x2 u; u.x = __builtin_amdgcn_exp2f(t.x); u.y = __builtin_amdgcn_exp2f(t.y);
  const f32x2 n = u * (u + 2.f), d = n + 2.f;
  f32x2 r; r.x = __builtin_amdgcn_rcpf(d.x); r.y = __builtin_amdgcn_rcpf(d.y);
  return (h * n) * r;
}

constexpr int G1_STAGE = 16384 + 28672, G1_NST = 3;
extern "C" __global__ __launch_bounds__(512, 2) void k4_gemm1(const u16* __restrict__ A, const u16* __restrict__ B, const u16* __restrict__ bias, u16* __restrict__ C) {
  extern __shared__ __attribute__((aligned(16))) char sm[];
  const int tid = threadIdx.x, lane = tid & 63, wv = __builtin_amdgcn_readfirstlane(tid >> 6);
  const u32 smbase = (u32)(size_t)(LDS_AS char*)sm;
  const int xcd = blockIdx.x & 7, idx = blockIdx.x >> 3;
  const int tm = (xcd & 1) * 8 + (idx & 7), tn = (xcd >> 1) * 4 + (idx >> 3);
  const int m0 = tm * 128, n0 = tn * 224;
  constexpr int NT = D_ / 64;
  if (wv >= 4) {
    const int w = wv - 4;
    const i32x4 rsA = mk_rsrc(A, S_ * D_ * 2);
    const i32x4 rsB = mk_rsrc(B, D_ * F_ * 2);
    int voA[4], voB[7];
#pragma unroll
    for (int i = 0; i < 4; ++i) { const int row = (w * 4 + i) * 8 + (lane >> 3), cch = (lane & 7) ^ (row & 7); voA[i] = (m0 + row) * (D_ * 2) + cch * 16; }
#pragma unroll
    for (int i = 0; i < 7; ++i) {
      const int L = (w * 7 + i) * 64 + lane, k = L / 28, cc = L % 28, nb = (cc >> 1) ^ ((k >> 3) & 1);
      voB[i] = k * (F_ * 2) + (n0 + nb * 16 + (cc & 1) * 8) * 2;
    }
#define G1_STAGE_LOAD(st, kt) do { const u32 sb_ = smbase + (st) * G1_STAGE; \
    _Pragma("unroll") for (int i = 0; i < 4; ++i) dma16(rsA, sb_ + (w * 4 + i) * 1024, voA[i], (kt) * 128); \
    _Pragma("unroll") for (int i = 0; i < 7; ++i) dma16(rsB, sb_ + 16384 + (w * 7 + i) * 1024, voB[i], (kt) * (64 * F_ * 2)); } while (0)
    G1_STAGE_LOAD(0, 0);
    G1_STAGE_LOAD(1, 1);
    G1_STAGE_LOAD(2, 2);
    WAIT_VM(22);
    BARRIER();
    int st = 0;
    for (int kt = 0; kt < NT; ++kt) {
      if (kt + 2 < NT) WAIT_VM(11); else WAIT_VM(0);
      BARRIER();
      if (kt + 3 < NT) G1_STAGE_LOAD(st, kt + 3);
      st = st == 2 ? 0 : st + 1;
    }
  } else {
    const int wr = wv >> 1, wc = wv & 1, fr = lane & 15, fq = lane >> 4;
    f32x4 acc[4][7];
#pragma unroll
    for (int i = 0; i < 4; ++i)
#pragma unroll
      for (int j = 0; j < 7; ++j) acc[i][j] = (f32x4){0.f, 0.f, 0.f, 0.f};
    const int aoff = (wr * 64 + fr) * 128;
    const int ach0 = ((0 + fq) ^ (fr & 7)) * 16, ach1 = ((4 + fq) ^ (fr & 7)) * 16;
    const int q = (lane & 15) >> 2, p = lane & 3;
    const int boff = 16384 + (8 * fq + q) * 448 + p * 8;
    const int gb = fq & 1;
    bf16x8 ax[4], bx[7], ay[4], by[7];
#define G1_READ(AF, BF, sb, ks) do { \
    _Pragma("unroll") for (int mi = 0; mi < 4; ++mi) AF[mi] = lds_a((sb) + aoff + mi * 2048 + ((ks) ? ach1 : ach0)); \
    _Pragma("unroll") for (int ni = 0; ni < 7; ++ni) { const char* bp = (sb) + boff + (ks) * (32 * 448) + (((wc * 7 + ni) ^ gb) * 32); BF[ni] = lds_tr(bp, bp + 4 * 448); } } while (0)
#define G1_MMA(AF, BF) do { \
    _Pragma("unroll") for (int ni = 0; ni < 7; ++ni) _Pragma("unroll") for (int mi = 0; mi < 4; ++mi) \
      acc[mi][ni] = __builtin_amdgcn_mfma_f32_16x16x32_bf16(BF[ni], AF[mi], acc[mi][ni], 0, 0, 0); } while (0)
#define G1_SCHED() do { _Pragma("unroll") for (int i = 0; i < 18; ++i) { __builtin_amdgcn_sched_group_barrier(0x008, 1, 0); __builtin_amdgcn_sched_group_barrier(0x100, 1, 0); } \
    __builtin_amdgcn_sched_group_barrier(0x008, 10, 0); __builtin_amdgcn_sched_barrier(0); } while (0)
    BARRIER();
    G1_READ(ax, bx, sm, 0);
    int st = 0;
    for (int kt = 0; kt < NT; ++kt) {
      const char* sb = sm + st * G1_STAGE;
      const int sn = st == 2 ? 0 : st + 1;
      __builtin_amdgcn_sched_barrier(0);
      G1_READ(ay, by, sb, 1);
      G1_MMA(ax, bx);
      G1_SCHED();
      BARRIER();
      __builtin_amdgcn_sched_barrier(0);
      G1_READ(ax, bx, sm + sn * G1_STAGE, 0);
      G1_MMA(ay, by);
      G1_SCHED();
      st = sn;
    }
#pragma unroll
    for (int ni = 0; ni < 7; ++ni)
#pragma unroll
      for (int mi = 0; mi < 4; ++mi) *(f32x4*)(sm + (wr * 64 + mi * 16 + fr) * 912 + (wc * 112 + ni * 16 + fq * 4) * 4) = acc[mi][ni];
  }
  BARRIER();
#pragma unroll
  for (int i = 0; i < 7; ++i) {
    const int id = tid + 512 * i, row = id / 28, ch = id % 28;
    const f32x4 v0 = *(const f32x4*)(sm + row * 912 + ch * 32), v1 = *(const f32x4*)(sm + row * 912 + ch * 32 + 16);
    const uint4 bb = *(const uint4*)(bias + n0 + ch * 8);
    const f32x2 r0 = mish2((f32x2){v0[0] + __uint_as_float(bb.x << 16), v0[1] + __uint_as_float(bb.x & 0xffff0000u)});
    const f32x2 r1 = mish2((f32x2){v0[2] + __uint_as_float(bb.y << 16), v0[3] + __uint_as_float(bb.y & 0xffff0000u)});
    const f32x2 r2 = mish2((f32x2){v1[0] + __uint_as_float(bb.z << 16), v1[1] + __uint_as_float(bb.z & 0xffff0000u)});
    const f32x2 r3 = mish2((f32x2){v1[2] + __uint_as_float(bb.w << 16), v1[3] + __uint_as_float(bb.w & 0xffff0000u)});
    uint4 o; o.x = pk2(r0.x, r0.y); o.y = pk2(r1.x, r1.y); o.z = pk2(r2.x, r2.y); o.w = pk2(r3.x, r3.y);
    gst16(C, S_ * LA_ * 2, (m0 + row) * (LA_ * 2) + n0 * 2 + ch * 16, __builtin_bit_cast(u32x4s, o));
  }
}

typedef u32 u32x4 __attribute__((ext_vector_type(4)));
constexpr int G2_STAGE = 32768, G2_NST = 4;
extern "C" __global__ __launch_bounds__(512, 2) void k5_gemm2(const u16* __restrict__ A, const u16* __restrict__ B, const u16* __restrict__ H, u16* __restrict__ C,
                                                   float* P, u32* flags) {
  extern __shared__ __attribute__((aligned(16))) char sm[];
  const int tid = threadIdx.x, lane = tid & 63, wv = __builtin_amdgcn_readfirstlane(tid >> 6);
  const u32 smbase = (u32)(size_t)(LDS_AS char*)sm;
  const int xcd = blockIdx.x & 7, idx = blockIdx.x >> 3, kh = xcd & 1, pair = (xcd >> 1) * 32 + idx;
  const int tm = (xcd >> 1) * 4 + (idx & 3), tn = idx >> 2;
  const int m0 = tm * 128, n0 = tn * 128, k0 = kh * (F_ / 2);
  constexpr int NT = F_ / 2 / 64;
  const __amdgpu_buffer_rsrc_t rp = __builtin_amdgcn_make_buffer_rsrc((void*)P, 0, 8 << 20, 0x00020000);
  const int fr = lane & 15, fq = lane >> 4;
  if (wv >= 4) {
    const int w = wv - 4;
    const i32x4 rsA = mk_rsrc(A, S_ * LA_ * 2);
    const i32x4 rsB = mk_rsrc(B, F_ * D_ * 2);
    int voA[4], voB[4];
#pragma unroll
    for (int i = 0; i < 4; ++i) { const int row = (w * 4 + i) * 8 + (lane >> 3), cch = (lane & 7) ^ (row & 7); voA[i] = (m0 + row) * (LA_ * 2) + k0 * 2 + cch * 16; }
#pragma unroll
    for (int i = 0; i < 4; ++i) {
      const int k = (w * 4 + i) * 4 + (lane >> 4), cc = lane & 15, f = (k & 3) | (((k >> 3) & 1) << 2), nb = (cc >> 1) ^ f;
      voB[i] = (k0 + k) * (D_ * 2) + (n0 + nb * 16 + (cc & 1) * 8) * 2;
    }
#define G2_STAGE_LOAD(st, kt) do { const u32 sb_ = smbase + (st) * G2_STAGE; \
    _Pragma("unroll") for (int i = 0; i < 4; ++i) dma16(rsA, sb_ + (w * 4 + i) * 1024, voA[i], (kt) * 128); \
    _Pragma("unroll") for (int i = 0; i < 4; ++i) dma16(rsB, sb_ + 16384 + (w * 4 + i) * 1024, voB[i], (kt) * (64 * D_ * 2)); } while (0)
    G2_STAGE_LOAD(0, 0);
    G2_STAGE_LOAD(1, 1);
    G2_STAGE_LOAD(2, 2);
    G2_STAGE_LOAD(3, 3);
    WAIT_VM(24);
    BARRIER();
    int st = 0;
    for (int kt = 0; kt < NT; ++kt) {
      if (kt + 3 < NT) WAIT_VM(16); else if (kt + 2 < NT) WAIT_VM(8); else WAIT_VM(0);
      BARRIER();
      if (kt + 4 < NT) G2_STAGE_LOAD(st, kt + 4);
      st = (st + 1) & 3;
    }
    BARRIER();
  } else {
    const int wr = wv >> 1, wc = wv & 1;
    f32x4 acc[4][4];
#pragma unroll
    for (int i = 0; i < 4; ++i)
#pragma unroll
      for (int j = 0; j < 4; ++j) acc[i][j] = (f32x4){0.f, 0.f, 0.f, 0.f};
    const int aoff = (wr * 64 + fr) * 128;
    const int ach0 = ((0 + fq) ^ (fr & 7)) * 16, ach1 = ((4 + fq) ^ (fr & 7)) * 16;
    const int q = (lane & 15) >> 2, p = lane & 3;
    const int bf_ = q | ((fq & 1) << 2);
    const int boff = 16384 + (8 * fq + q) * 256 + p * 8;
    bf16x8 ax[4], bx[4], ay[4], by[4];
#define G2_READ(AF, BF, sb, ks) do { \
    _Pragma("unroll") for (int mi = 0; mi < 4; ++mi) AF[mi] = lds_a((sb) + aoff + mi * 2048 + ((ks) ? ach1 : ach0)); \
    _Pragma("unroll") for (int ni = 0; ni < 4; ++ni) { const char* bp = (sb) + boff + (ks) * 8192 + (((wc * 4 + ni) ^ bf_) * 32); BF[ni] = lds_tr(bp, bp + 1024); } } while (0)
#define G2_MMA(AF, BF) do { \
    _Pragma("unroll") for (int ni = 0; ni < 4; ++ni) _Pragma("unroll") for (int mi = 0; mi < 4; ++mi) \
      acc[mi][ni] = __builtin_amdgcn_mfma_f32_16x16x32_bf16(BF[ni], AF[mi], acc[mi][ni], 0, 0, 0); } while (0)
#define G2_SCHED() do { _Pragma("unroll") for (int i = 0; i < 12; ++i) { __builtin_amdgcn_sched_group_barrier(0x008, 1, 0); __builtin_amdgcn_sched_group_barrier(0x100, 1, 0); } \
    __builtin_amdgcn_sched_group_barrier(0x008, 4, 0); __builtin_amdgcn_sched_barrier(0); } while (0)
    BARRIER();
    G2_READ(ax, bx, sm, 0);
    int st = 0;
    for (int kt = 0; kt < NT; ++kt) {
      const char* sb = sm + st * G2_STAGE;
      const int sn = (st + 1) & 3;
      __builtin_amdgcn_sched_barrier(0);
      G2_READ(ay, by, sb, 1);
      G2_MMA(ax, bx);
      G2_SCHED();
      BARRIER();
      __builtin_amdgcn_sched_barrier(0);
      G2_READ(ax, bx, sm + sn * G2_STAGE, 0);
      G2_MMA(ay, by);
      G2_SCHED();
      st = sn;
    }
    if (wr != kh) {
      const int base = ((pair * 2 + kh) * 2 + wc) * 16384 + lane * 16;
#pragma unroll
      for (int mi = 0; mi < 4; ++mi)
#pragma unroll
        for (int ni = 0; ni < 4; ++ni) __builtin_amdgcn_raw_buffer_store_b128(__builtin_bit_cast(u32x4, acc[mi][ni]), rp, base + (mi * 4 + ni) * 1024, 0, 16);
      WAIT_VM(0);
      if (lane == 0) __hip_atomic_store(flags + ((pair * 2 + kh) * 2 + wc) * 16, 1u, __ATOMIC_RELAXED, __HIP_MEMORY_SCOPE_AGENT);
      BARRIER();
    } else {
      u32* fl = flags + ((pair * 2 + (kh ^ 1)) * 2 + wc) * 16;
      for (int spin = 0; spin < (1 << 20); ++spin) {
        if (__hip_atomic_load(fl, __ATOMIC_RELAXED, __HIP_MEMORY_SCOPE_AGENT) != 0u) break;
        __builtin_amdgcn_s_sleep(1);
      }
      BARRIER();
      const int base = ((pair * 2 + (kh ^ 1)) * 2 + wc) * 16384 + lane * 16;
      u32x4 pv[16];
#pragma unroll
      for (int i = 0; i < 16; ++i) pv[i] = __builtin_amdgcn_raw_buffer_load_b128(rp, base + i * 1024, 0, 16);
#pragma unroll
      for (int mi = 0; mi < 4; ++mi)
#pragma unroll
        for (int ni = 0; ni < 4; ++ni)
          *(f32x4*)(sm + (mi * 16 + fr) * 528 + (wc * 64 + ni * 16 + fq * 4) * 4) = acc[mi][ni] + __builtin_bit_cast(f32x4, pv[mi * 4 + ni]);
    }
  }
  const int mo = m0 + kh * 64;
  uint4 hv[2];
#pragma unroll
  for (int i = 0; i < 2; ++i) { const int id = tid + 512 * i, row = id >> 4, ch = id & 15; hv[i] = *(const uint4*)(H + (size_t)(mo + row) * D_ + n0 + ch * 8); }
  BARRIER();
#pragma unroll
  for (int i = 0; i < 2; ++i) {
    const int id = tid + 512 * i, row = id >> 4, ch = id & 15;
    const f32x4 s0 = *(const f32x4*)(sm + row * 528 + ch * 32), s1 = *(const f32x4*)(sm + row * 528 + ch * 32 + 16);
    uint4 o;
    o.x = pk2(__uint_as_float(hv[i].x << 16) + bfr(s0[0]), __uint_as_float(hv[i].x & 0xffff0000u) + bfr(s0[1]));
    o.y = pk2(__uint_as_float(hv[i].y << 16) + bfr(s0[2]), __uint_as_float(hv[i].y & 0xffff0000u) + bfr(s0[3]));
    o.z = pk2(__uint_as_float(hv[i].z << 16) + bfr(s1[0]), __uint_as_float(hv[i].z & 0xffff0000u) + bfr(s1[1]));
    o.w = pk2(__uint_as_float(hv[i].w << 16) + bfr(s1[2]), __uint_as_float(hv[i].w & 0xffff0000u) + bfr(s1[3]));
    *(uint4*)(C + (size_t)(mo + row) * D_ + n0 + ch * 8) = o;
  }
}

extern "C" void kernel_launch(void* const* d_in, const int* in_sizes, int n_in, void* d_out, int out_size, void* d_ws, size_t ws_size,
                              hipStream_t stream) {
  const u16* x = (const u16*)d_in[0];
  const u16* wr = (const u16*)d_in[1];
  const u16* wi = (const u16*)d_in[2];
  const u16* w1 = (const u16*)d_in[4];
  const u16* b1 = (const u16*)d_in[5];
  const u16* w2 = (const u16*)d_in[6];
  char* ws = (char*)d_ws;
  float2* T = (float2*)ws;
  u32* Wt = (u32*)(ws + (8u << 20));
  u16* h = (u16*)(ws + (12u << 20));
  u16* ln2 = (u16*)(ws + (16u << 20));
  u16* act = (u16*)(ws + (20u << 20));
  float4* Cs = (float4*)(ws + (36u << 20));
  u32* flags = (u32*)(ws + (37u << 20));
  float* P = (float*)(ws + (40u << 20));
  (void)hipFuncSetAttribute((const void*)k4_gemm1, hipFuncAttributeMaxDynamicSharedMemorySize, G1_STAGE * G1_NST);
  (void)hipFuncSetAttribute((const void*)k5_gemm2, hipFuncAttributeMaxDynamicSharedMemorySize, G2_STAGE * G2_NST);
  k1_ln_rfft<<<512, 512, 0, stream>>>(x, wr, wi, T, Wt, Cs, flags);
  k2_col<<<256, 512, 0, stream>>>(T, Wt, Cs, (const u32*)w2);
  k3_irfft_ln<<<256, 512, 0, stream>>>(T, x, h, ln2, (const u32*)w1);
  k4_gemm1<<<256, 512, G1_STAGE * G1_NST, stream>>>(ln2, w1, b1, act);
  k5_gemm2<<<256, 512, G2_STAGE * G2_NST, stream>>>(act, w2, h, (u16*)d_out, P, flags);
}
```
